```python
import torch

def _softmax(x, axis=-1):
    return torch.softmax(x, dim=axis)
def _gelu(x):
    return torch.nn.functional.gelu(x, approximate="tanh")
def _silu(x):
    return torch.nn.functional.silu(x)
def _sigmoid(x):
    return torch.sigmoid(x)
def _layernorm(x, axis=-1, eps=1e-5):
    m = x.mean(dim=axis, keepdim=True)
    v = x.var(dim=axis, keepdim=True, unbiased=False)
    return (x - m) / torch.sqrt(v + eps)
def _scatter_add(tgt, idx, src, axis=0):
    return tgt.clone().index_add_(axis, idx, src)
def _segment_sum(src, seg, n):
    out = torch.zeros((n,) + src.shape[1:], dtype=src.dtype)
    return out.index_add_(0, seg, src)
def _pad(x, pw):
    flat = tuple(v for pair in reversed(pw) for v in pair)
    return torch.nn.functional.pad(x, flat)

kernel_name = 'parallel_sum__spectral_gfnet__dense_activation_gelu__prompt'

def setup_inputs():

    _w = (lambda a: (lambda t: t.to(torch.bfloat16) if t.is_floating_point() else t)(torch.as_tensor(a)))
    def _A_setup():
        B, S, D = 1, 2048, 448
        Df = D // 2 + 1
        return {
            "x": _w((lambda *s: torch.randn(*s, dtype=torch.bfloat16) * 0.05)(B, S, D)),
            "w_r": _w((lambda *s: torch.randn(*s, dtype=torch.bfloat16) * 0.8)(S, Df) * 0.02),
            "w_i": _w((lambda *s: torch.randn(*s, dtype=torch.bfloat16) * 0.8)(S, Df) * 0.02),
        }
    _A_in = _A_setup()

    def _B_setup():
        B, S, D, F = 1, 2048, 448, 1792
        return {
            "x": _w((lambda *s: torch.randn(*s, dtype=torch.bfloat16) * 0.05)(B, S, D)),
            "w1": _w((lambda *s: torch.randn(*s, dtype=torch.bfloat16) * 0.4)(D, F) * (D ** -0.5)),
            "b1": _w((lambda *s: torch.randn(*s, dtype=torch.bfloat16) * 12.8)(F) * 0.01),
            "w2": _w((lambda *s: torch.randn(*s, dtype=torch.bfloat16) * 0.2)(F, D) * (F ** -0.5)),
            "b2": _w((lambda *s: torch.randn(*s, dtype=torch.bfloat16) * 1.6)(D) * 0.01),
        }
    _B_in = _B_setup()

    return {
        "A_x": _A_in["x"],
        "A_w_r": _A_in["w_r"],
        "A_w_i": _A_in["w_i"],
        "B_x": _B_in["x"],
        "B_w1": _B_in["w1"],
        "B_b1": _B_in["b1"],
        "B_w2": _B_in["w2"],
        "B_b2": _B_in["b2"],
    }

def reference(A_x, A_w_r, A_w_i, B_x, B_w1, B_b1, B_w2, B_b2):
    def _A_ref(x, w_r, w_i):
        B, S, D = x.shape
        X = (lambda x, axes=(-2,-1): torch.fft.rfft2(x.float(), dim=axes))(x, axes=(-2, -1))
        W = w_r + 1j * w_i
        X = X * W
        y = (lambda x, s, axes=(-2,-1): torch.fft.irfft2(x, s=s, dim=axes).to(torch.bfloat16))(X, (S, D), axes=(-2, -1))
        return y

    def _B_ref(x, w1, b1, w2, b2):
        h = torch.einsum("bsd,df->bsf", x, w1) + b1
        h = _gelu(h)
        return torch.einsum("bsf,fd->bsd", h, w2) + b2

    x = A_x
    xn = _layernorm(x, axis=-1)
    return x + _A_ref(xn, A_w_r, A_w_i) + _B_ref(xn, B_w1, B_b1, B_w2, B_b2)
```

```cpp
#include <hip/hip_runtime.h>
#include <hip/hip_bf16.h>
#include <math.h>

constexpr int S = 2048, D = 448, DF = 225, F = 1792;

__device__ __forceinline__ float bf2f(unsigned short u) { return __uint_as_float(((unsigned)u) << 16); }
__device__ __forceinline__ unsigned short f2bf(float f) { __hip_bfloat16 b = __float2bfloat16(f); return *(unsigned short*)&b; }
__device__ __forceinline__ float rbf(float f) { return bf2f(f2bf(f)); }

__global__ void k_ln(const unsigned short* x, float* xn) {
  int r = blockIdx.x; int t = threadIdx.x;
  float v[7]; float s = 0;
  for (int i = 0; i < 7; i++) { v[i] = bf2f(x[r * D + t + 64 * i]); s += v[i]; }
  for (int o = 32; o; o >>= 1) s += __shfl_xor(s, o);
  float mean = s / D; float q = 0;
  for (int i = 0; i < 7; i++) { float d = v[i] - mean; q += d * d; }
  for (int o = 32; o; o >>= 1) q += __shfl_xor(q, o);
  float var = rbf(q / D); float m = rbf(mean);
  float sd = rbf(sqrtf(rbf(var + 1e-5f)));
  for (int i = 0; i < 7; i++) xn[r * D + t + 64 * i] = rbf(rbf(v[i] - m) / sd);
}
__global__ void k_rowdft(const float* xn, float2* X1) {
  __shared__ float row[D]; __shared__ float2 tw[D];
  int r = blockIdx.x; int t = threadIdx.x;
  for (int i = t; i < D; i += blockDim.x) { row[i] = xn[r * D + i]; float sn, cs; sincospif(-2.0f * i / D, &sn, &cs); tw[i] = make_float2(cs, sn); }
  __syncthreads();
  if (t < DF) {
    float re = 0, im = 0; int idx = 0;
    for (int d = 0; d < D; d++) { re += row[d] * tw[idx].x; im += row[d] * tw[idx].y; idx += t; if (idx >= D) idx -= D; }
    X1[t * S + r] = make_float2(re, im);
  }
}
__global__ void k_col(const float2* X1, const unsigned short* wr, const unsigned short* wi, float2* Y1) {
  __shared__ float2 z[S]; __shared__ float2 Z[S]; __shared__ float2 tw[S];
  int k = blockIdx.x; int t = threadIdx.x;
  for (int i = t; i < S; i += blockDim.x) { z[i] = X1[k * S + i]; float sn, cs; sincospif(-2.0f * i / S, &sn, &cs); tw[i] = make_float2(cs, sn); }
  __syncthreads();
  for (int f = t; f < S; f += blockDim.x) {
    float re = 0, im = 0;
    for (int s = 0; s < S; s++) { float2 w = tw[(s * f) & (S - 1)]; re += z[s].x * w.x - z[s].y * w.y; im += z[s].x * w.y + z[s].y * w.x; }
    float a = bf2f(wr[f * DF + k]), b = bf2f(wi[f * DF + k]);
    Z[f] = make_float2(re * a - im * b, re * b + im * a);
  }
  __syncthreads();
  for (int s = t; s < S; s += blockDim.x) {
    float re = 0, im = 0;
    for (int f = 0; f < S; f++) { float2 w = tw[(s * f) & (S - 1)]; re += Z[f].x * w.x + Z[f].y * w.y; im += -Z[f].x * w.y + Z[f].y * w.x; }
    Y1[s * DF + k] = make_float2(re, im);
  }
}
__global__ void k_h(const float* xn, const unsigned short* w1, const unsigned short* b1, float* h) {
  __shared__ float row[D];
  int r = blockIdx.x; int t = threadIdx.x;
  for (int i = t; i < D; i += blockDim.x) row[i] = xn[r * D + i];
  __syncthreads();
  for (int f = t; f < F; f += blockDim.x) {
    float a = 0;
    for (int d = 0; d < D; d++) a += row[d] * bf2f(w1[d * F + f]);
    float u = rbf(rbf(a) + bf2f(b1[f]));
    float g = 0.5f * u * (1.0f + tanhf(0.7978845608028654f * (u + 0.044715f * u * u * u)));
    h[r * F + f] = rbf(g);
  }
}
__global__ void k_out(const unsigned short* x, const float2* Y1, const float* h, const unsigned short* w2, const unsigned short* b2, unsigned short* out) {
  __shared__ float row[F]; __shared__ float2 y[DF]; __shared__ float2 tw[D];
  int r = blockIdx.x; int t = threadIdx.x;
  for (int i = t; i < F; i += blockDim.x) row[i] = h[r * F + i];
  for (int i = t; i < DF; i += blockDim.x) y[i] = Y1[r * DF + i];
  for (int i = t; i < D; i += blockDim.x) { float sn, cs; sincospif(2.0f * i / D, &sn, &cs); tw[i] = make_float2(cs, sn); }
  __syncthreads();
  for (int d = t; d < D; d += blockDim.x) {
    float a = 0;
    for (int f = 0; f < F; f++) a += row[f] * bf2f(w2[f * D + d]);
    float yb = rbf(rbf(a) + bf2f(b2[d]));
    float ya = y[0].x + ((d & 1) ? -y[224].x : y[224].x);
    int idx = d;
    for (int k = 1; k < 224; k++) { ya += 2.0f * (y[k].x * tw[idx].x - y[k].y * tw[idx].y); idx += d; if (idx >= D) idx -= D; }
    ya = rbf(ya * (1.0f / (S * D)));
    out[r * D + d] = f2bf(rbf(bf2f(x[r * D + d]) + ya) + yb);
  }
}

extern "C" void kernel_launch(void* const* d_in, const int* in_sizes, int n_in,
                              void* d_out, int out_size, void* d_ws, size_t ws_size,
                              hipStream_t stream) {
  const unsigned short* x = (const unsigned short*)d_in[0];
  const unsigned short* wr = (const unsigned short*)d_in[1];
  const unsigned short* wi = (const unsigned short*)d_in[2];
  const unsigned short* w1 = (const unsigned short*)d_in[4];
  const unsigned short* b1 = (const unsigned short*)d_in[5];
  const unsigned short* w2 = (const unsigned short*)d_in[6];
  const unsigned short* b2 = (const unsigned short*)d_in[7];
  char* ws = (char*)d_ws;
  float* xn = (float*)ws;
  float2* X1 = (float2*)(ws + (8 << 20));
  float2* Y1 = (float2*)(ws + (16 << 20));
  float* h = (float*)(ws + (24 << 20));
  k_ln<<<S, 64, 0, stream>>>(x, xn);
  k_rowdft<<<S, 256, 0, stream>>>(xn, X1);
  k_col<<<DF, 1024, 0, stream>>>(X1, wr, wi, Y1);
  k_h<<<S, 256, 0, stream>>>(xn, w1, b1, h);
  k_out<<<S, 256, 0, stream>>>(x, Y1, h, w2, b2, (unsigned short*)d_out);
}
```

```cpp
#include <hip/hip_runtime.h>
#include <stdint.h>

typedef __bf16 bf2 __attribute__((ext_vector_type(2)));
typedef __bf16 bf4 __attribute__((ext_vector_type(4)));
typedef __bf16 bf8 __attribute__((ext_vector_type(8)));
typedef float f2 __attribute__((ext_vector_type(2)));
typedef float f4 __attribute__((ext_vector_type(4)));
typedef float f16v __attribute__((ext_vector_type(16)));
typedef unsigned u2 __attribute__((ext_vector_type(2)));
typedef unsigned u4 __attribute__((ext_vector_type(4)));
typedef unsigned long long u64;
#define LAS __attribute__((address_space(3)))
#define DI __device__ __forceinline__
typedef __amdgpu_buffer_rsrc_t rsrc_t;

constexpr int WS_WT = 65536;
constexpr int WS_X1 = WS_WT + 232 * 8192;
constexpr int WS_Y1 = WS_X1 + 224 * 8192;
constexpr int WS_H = WS_Y1 + 224 * 8192;
constexpr int WS_END = WS_H + 32 * 112 * 2048;
constexpr int AUX = 0;

#define STAMP(w, i)

struct Params {
  const char* x; const char* wr; const char* wi; const char* w1; const char* b1; const char* w2; const char* b2;
  char* out; char* ws;
};

DI unsigned pk(float a, float b) { f2 v = {a, b}; bf2 r = __builtin_convertvector(v, bf2); return __builtin_bit_cast(unsigned, r); }
DI float rbf(float a) { return __uint_as_float(pk(a, a) << 16); }
DI float bflo(unsigned u) { return __uint_as_float(u << 16); }
DI float bfhi(unsigned u) { return __uint_as_float(u & 0xffff0000u); }
DI bf8 asbf8(u4 v) { return __builtin_bit_cast(bf8, v); }
DI u4 asu4(bf8 v) { return __builtin_bit_cast(u4, v); }
DI float dot2(unsigned a, unsigned b, float c) { return __builtin_amdgcn_fdot2_f32_bf16(__builtin_bit_cast(bf2, a), __builtin_bit_cast(bf2, b), c, false); }
DI f16v mfma32(bf8 a, bf8 b, f16v c) { return __builtin_amdgcn_mfma_f32_32x32x16_bf16(a, b, c, 0, 0, 0); }
DI f4 mfma16(bf8 a, bf8 b, f4 c) { return __builtin_amdgcn_mfma_f32_16x16x32_bf16(a, b, c, 0, 0, 0); }
DI bf8 lds128(unsigned a) { return *(const LAS bf8*)(uintptr_t)a; }
DI bf4 ldstr(unsigned a) { return __builtin_amdgcn_ds_read_tr16_b64_v4bf16((LAS bf4*)(uintptr_t)a); }
DI bf8 cat(bf4 a, bf4 b) { return __builtin_shufflevector(a, b, 0, 1, 2, 3, 4, 5, 6, 7); }
DI float xhalf(float v) { unsigned u = __float_as_uint(v); u2 r = __builtin_amdgcn_permlane32_swap(u, u, false, false); unsigned a = r[0], b = r[1]; return __uint_as_float(a) + __uint_as_float(b); }
DI float cosr(float rev) { return __builtin_amdgcn_cosf(rev); }
DI float sinr(float rev) { return __builtin_amdgcn_sinf(rev); }
DI void glds16(unsigned voff, const char* sbase, unsigned lds) {
  unsigned keep;
  asm volatile("s_mov_b32 %0, m0\n\ts_mov_b32 m0, %3\n\ts_nop 0\n\tglobal_load_lds_dwordx4 %1, %2\n\ts_mov_b32 m0, %0" : "=&s"(keep) : "v"(voff), "s"(sbase), "s"(lds) : "memory");
}
DI void glds4(unsigned voff, const char* sbase, unsigned lds) {
  unsigned keep;
  asm volatile("s_mov_b32 %0, m0\n\ts_mov_b32 m0, %3\n\ts_nop 0\n\tglobal_load_lds_dword %1, %2\n\ts_mov_b32 m0, %0" : "=&s"(keep) : "v"(voff), "s"(sbase), "s"(lds) : "memory");
}
#define WAITV(n) asm volatile("s_waitcnt vmcnt(" #n ")" ::: "memory")
#define WAITL0() asm volatile("s_waitcnt lgkmcnt(0)" ::: "memory")
#define BAR() do { asm volatile("" ::: "memory"); __builtin_amdgcn_s_barrier(); asm volatile("" ::: "memory"); } while (0)
#define SCHED() __builtin_amdgcn_sched_barrier(0)

constexpr unsigned L_DFT = 131072;
constexpr unsigned L_B1 = L_DFT + 28672;
constexpr unsigned L_TOTAL = 163840;

DI u4 gelu8(const f16v& a, int o, float r) {
  float g[8];
#pragma unroll
  for (int i = 0; i < 8; i++) {
    float u = a[o + i] * r;
    float z = u * __builtin_fmaf(u * u, -0.10294324f, -2.3022082f);
    g[i] = u * __builtin_amdgcn_rcpf(1.0f + __builtin_amdgcn_exp2f(z));
  }
  return u4{pk(g[0], g[1]), pk(g[2], g[3]), pk(g[4], g[5]), pk(g[6], g[7])};
}
DI bf8 split3(float c, unsigned bias) {
  float hi = rbf(c), mid = rbf(c - hi), lo = c - hi - mid;
  return asbf8(u4{pk(hi, mid), (pk(lo, lo) & 0xffffu) | (bias << 16), 0u, 0u});
}

template <int G> struct Frag { bf8 x; bf8 w[G == 0 ? 3 : 2]; };

template <int G>
DI void s1_body(const Params& p, int rt, int ft, int s2o, int cto, int w, int l) {
  const int sg = w & 3, h = l >> 5;
  unsigned xoff[2], woff[2];
#pragma unroll
  for (int j = 0; j < 2; j++) {
    int row = 16 * w + 8 * j + (l >> 3);
    xoff[j] = (32 * (row & 63) + 16 * (row >> 6) + rt) * 896 + (((l & 7) ^ ((row >> 1) & 7)) << 4);
    int kr = 8 * w + 4 * j + (l >> 4);
    woff[j] = kr * 3584 + ft * 256 + (((l & 15) ^ ((kr & 3) << 2)) << 4);
  }
  const unsigned w2off = (224 * w + (l >> 3)) * 896 + cto * 128 + (((l & 7) ^ (((l >> 4) & 1) << 2)) << 4);
  if (w == 0) glds4(l * 4, p.b1 + ft * 256, L_B1);
#pragma unroll
  for (int c = 0; c < 4; c++) {
#pragma unroll
    for (int j = 0; j < 2; j++) glds16(xoff[j], p.x + c * 128, c * 32768 + (2 * w + j) * 1024);
#pragma unroll
    for (int j = 0; j < 2; j++) glds16(woff[j], p.w1 + c * 64 * 3584, c * 32768 + 16384 + (2 * w + j) * 1024);
  }
  {
    int c = l & 31, k = 16 * ft + (c & 15);
    float ph = (c >> 4) ? 0.25f : 0.0f;
    if (k == 0 && c == 16) { k = 224; ph = 0.f; }
    const float dk = (float)k * (1.0f / 448.0f);
    for (int st = w; st < 28; st += 8) {
      int b0 = ((16 * st + 8 * h) * k) % 448;
      float a0 = __builtin_fmaf((float)b0, 1.0f / 448.0f, ph);
      float v[8];
#pragma unroll
      for (int j = 0; j < 8; j++) v[j] = cosr(__builtin_fmaf((float)j, dk, a0));
      *(LAS u4*)(uintptr_t)(L_DFT + st * 1024 + l * 16) = u4{pk(v[0], v[1]), pk(v[2], v[3]), pk(v[4], v[5]), pk(v[6], v[7])};
    }
  }
  unsigned xa[4], wa[G == 0 ? 3 : 1];
  {
    int row = 32 * sg + (l & 31);
#pragma unroll
    for (int v = 0; v < 4; v++) xa[v] = row * 128 + (((2 * v + h) ^ ((row >> 1) & 7)) << 4);
    int q = (l >> 2) & 3, pp = l & 3, fh = (l >> 4) & 1;
#pragma unroll
    for (int t = 0; t < (G == 0 ? 3 : 1); t++) {
      int T = G == 0 ? t : 3;
      wa[t] = 16384 + (8 * h + q) * 256 + (((4 * T + 2 * fh + (pp >> 1)) ^ (q << 2)) << 4) + (pp & 1) * 8;
    }
  }
  f16v acc[G == 0 ? 3 : 2];
#pragma unroll
  for (int t = 0; t < (G == 0 ? 3 : 2); t++)
#pragma unroll
    for (int i = 0; i < 16; i++) acc[t][i] = 0.f;
  float sx0 = 0.f, sx1 = 0.f, sq0 = 0.f, sq1 = 0.f, cs[G == 0 ? 3 : 2];
#pragma unroll
  for (int t = 0; t < (G == 0 ? 3 : 2); t++) cs[t] = 0.f;

  auto load = [&](Frag<G>& f, unsigned base, unsigned dbase, int v) {
    f.x = lds128(base + xa[v]);
#pragma unroll
    for (int t = 0; t < (G == 0 ? 3 : 1); t++) f.w[t] = cat(ldstr(base + wa[t] + v * 4096), ldstr(base + wa[t] + v * 4096 + 1024));
    if (G == 1) f.w[1] = lds128(dbase + v * 1024);
  };
  auto mma = [&](const Frag<G>& f, int v) {
    if (G == 0) {
#pragma unroll
      for (int t = 0; t < 3; t++) acc[t] = mfma32(f.w[t], f.x, acc[t]);
    } else {
      acc[0] = mfma32(f.w[0], f.x, acc[0]);
      acc[1] = mfma32(f.x, f.w[1], acc[1]);
    }
    u4 xu = asu4(f.x);
    sx0 = dot2(xu[0], 0x3f803f80u, sx0); sq0 = dot2(xu[0], xu[0], sq0);
    sx1 = dot2(xu[1], 0x3f803f80u, sx1); sq1 = dot2(xu[1], xu[1], sq1);
    sx0 = dot2(xu[2], 0x3f803f80u, sx0); sq0 = dot2(xu[2], xu[2], sq0);
    sx1 = dot2(xu[3], 0x3f803f80u, sx1); sq1 = dot2(xu[3], xu[3], sq1);
    if (v == sg) {
#pragma unroll
      for (int i = 0; i < 4; i++)
#pragma unroll
        for (int t = 0; t < (G == 0 ? 3 : 1); t++) cs[t] = dot2(asu4(f.w[t])[i], 0x3f803f80u, cs[t]);
    }
    if (G == 1) {
#pragma unroll
      for (int i = 0; i < 4; i++) cs[1] = dot2(asu4(f.w[1])[i], 0x3f803f80u, cs[1]);
    }
  };
  STAMP(0, 1);
  asm volatile("s_waitcnt vmcnt(12) lgkmcnt(0)" ::: "memory");
  BAR();
  STAMP(0, 2);
  Frag<G> fa, fb;
  load(fa, 0, L_DFT + l * 16, 0);
#pragma unroll 1
  for (int c = 0; c < 7; c++) {
    const unsigned base = (c & 3) * 32768, db = L_DFT + l * 16 + c * 4096;
    load(fb, base, db, 1); mma(fa, 0); SCHED();
    load(fa, base, db, 2); mma(fb, 1); SCHED();
    load(fb, base, db, 3); mma(fa, 2); SCHED();
    if (c < 6) asm volatile("s_waitcnt vmcnt(8) lgkmcnt(0)" ::: "memory"); else WAITL0();
    BAR();
    if (c < 3) {
#pragma unroll
      for (int j = 0; j < 2; j++) glds16(xoff[j], p.x + (c + 4) * 128, base + (2 * w + j) * 1024);
#pragma unroll
      for (int j = 0; j < 2; j++) glds16(woff[j], p.w1 + (c + 4) * 64 * 3584, base + 16384 + (2 * w + j) * 1024);
    } else {
#pragma unroll
      for (int j = 0; j < 4; j++) glds16(w2off, p.w2 + ((c - 3) * 32 + j * 8) * 896, base + w * 4096 + j * 1024);
    }
    if (c < 6) load(fa, ((c + 1) & 3) * 32768, db + 4096, 0);
    mma(fb, 3); SCHED();
  }
  STAMP(0, 3);
#pragma unroll
  for (int t = 0; t < (G == 0 ? 3 : 1); t++) *(LAS float*)(uintptr_t)(L_DFT + ((sg * 4 + (G == 0 ? t : 3)) * 64 + l) * 4) = cs[t];
  const float sum = xhalf(sx0 + sx1), ssq = xhalf(sq0 + sq1);
  const float mean = sum * (1.0f / 448.0f);
  const float var = __builtin_fmaxf(__builtin_fmaf(-mean, mean, ssq * (1.0f / 448.0f)), 0.f);
  const float sd = rbf(__builtin_sqrtf(rbf(rbf(var) + 1e-5f)));
  const float r = 1.0f / sd;
  const bf8 ext = asbf8(u4{pk(-mean, -mean), pk(-mean, sd), 0u, 0u});
  if (G == 1 && h == 0) *(LAS float*)(uintptr_t)(L_DFT + 4096 + sg * 128 + l * 4) = r;
  WAITL0();
  BAR();
  const rsrc_t rws = __builtin_amdgcn_make_buffer_rsrc(p.ws, 0, WS_END, 0x00020000);
  const int s2 = 16 * (sg >> 1) + rt;
  if (G == 1) {
    acc[1] = mfma32(ext, split3(cs[1], 0u), acc[1]);
    const int c = l & 31;
    const unsigned o = WS_X1 + (16 * ft + (c & 15)) * 8192 + s2 * 256 + (sg & 1) * 128 + (c >> 4) * 32 + h * 16;
#pragma unroll
    for (int t = 0; t < 2; t++) {
      f4 r0 = *(const LAS f4*)(uintptr_t)(L_DFT + 4096 + sg * 128 + (16 * t + 4 * h) * 4);
      f4 r1 = *(const LAS f4*)(uintptr_t)(L_DFT + 4096 + sg * 128 + (16 * t + 8 + 4 * h) * 4);
      const f16v& a = acc[1];
      u4 v = {pk(a[8 * t] * r0[0], a[8 * t + 1] * r0[1]), pk(a[8 * t + 2] * r0[2], a[8 * t + 3] * r0[3]),
              pk(a[8 * t + 4] * r1[0], a[8 * t + 5] * r1[1]), pk(a[8 * t + 6] * r1[2], a[8 * t + 7] * r1[3])};
      __builtin_amdgcn_raw_buffer_store_b128(v, rws, o + t * 64, 0, AUX);
    }
  }
#pragma unroll
  for (int t = 0; t < (G == 0 ? 3 : 1); t++) {
    const int T = G == 0 ? t : 3;
    float c = 0.f;
#pragma unroll
    for (int s = 0; s < 4; s++) c += *(const LAS float*)(uintptr_t)(L_DFT + ((s * 4 + T) * 64 + l) * 4);
    unsigned bias = *(const LAS unsigned short*)(uintptr_t)(L_B1 + (T * 32 + (l & 31)) * 2);
    acc[t] = mfma32(split3(c, h ? 0u : bias), ext, acc[t]);
  }
#pragma unroll
  for (int t = 0; t < (G == 0 ? 3 : 1); t++) {
    const int T = G == 0 ? t : 3;
    const unsigned o = WS_H + (((s2 * 112 + ft * 8 + T * 2) * 2 + (sg & 1)) * 64 + l) * 16;
    __builtin_amdgcn_raw_buffer_store_b128(gelu8(acc[t], 0, r), rws, o, 0, AUX);
    __builtin_amdgcn_raw_buffer_store_b128(gelu8(acc[t], 8, r), rws, o + 2048, 0, AUX);
  }
  STAMP(0, 4);
}

DI void helper_wt(const Params& p, int id) {
  const int tid = threadIdx.x;
  const int w4 = id >> 3, f20 = (id & 7) * 4;
  for (int e = tid; e < 3600; e += 512) {
    int seg = e / 450, o = e - seg * 450;
    const char* src = (seg >= 4 ? p.wi : p.wr) + (size_t)(16 * w4 + 64 * (f20 + (seg & 3))) * 450 + o * 16;
    *(LAS u4*)(uintptr_t)(e * 16) = *(const u4*)src;
  }
  __syncthreads();
  const rsrc_t rws = __builtin_amdgcn_make_buffer_rsrc(p.ws, 0, WS_END, 0x00020000);
  for (int e = tid; e < 3600; e += 512) {
    int k = e >> 4, c = e & 15;
    u4 v;
#pragma unroll
    for (int i = 0; i < 4; i++) {
      unsigned a = *(const LAS unsigned short*)(uintptr_t)(i * 7200 + c * 450 + k * 2);
      unsigned b = *(const LAS unsigned short*)(uintptr_t)(28800 + i * 7200 + c * 450 + k * 2);
      v[i] = a | (b << 16);
    }
    __builtin_amdgcn_raw_buffer_store_b128(v, rws, WS_WT + k * 8192 + id * 256 + c * 16, 0, AUX);
  }
}

__global__ __launch_bounds__(512, 2) void k_s1(Params p) {
  extern __shared__ __attribute__((aligned(16))) char smem[];
  STAMP(0, 0);
  const int b = blockIdx.x, xc = b & 7, i = b >> 3;
  const int l = threadIdx.x & 63, w = __builtin_amdgcn_readfirstlane(threadIdx.x >> 6);
  if (i >= 28) { helper_wt(p, xc * 4 + (i - 28)); return; }
  const int rt = (xc >> 1) * 4 + i / 7, ft = (xc & 1) * 7 + i % 7;
  const int s2o = xc * 4 + i / 7, cto = i % 7;
  if (w < 4) s1_body<0>(p, rt, ft, s2o, cto, w, l); else s1_body<1>(p, rt, ft, s2o, cto, w, l);
  WAITV(0);
  STAMP(0, 5);
}

DI bf8 gen8(float a0, float da_lo, float da_hi) {
  float v[8];
#pragma unroll
  for (int j = 0; j < 8; j++) v[j] = cosr(a0 + (float)(j & 3) * da_lo + (float)(j >> 2) * da_hi);
  return asbf8(u4{pk(v[0], v[1]), pk(v[2], v[3]), pk(v[4], v[5]), pk(v[6], v[7])});
}
DI bf8 neg8(bf8 a) { u4 v = asu4(a); return asbf8(v ^ 0x80008000u); }
DI bf8 pack44(f4 a, f4 b) { return asbf8(u4{pk(a[0], a[1]), pk(a[2], a[3]), pk(b[0], b[1]), pk(b[2], b[3])}); }

DI void fft_col(const Params& p, int k, int w4, int l, unsigned lds) {
  const rsrc_t rws = __builtin_amdgcn_make_buffer_rsrc(p.ws, 0, WS_END, 0x00020000);
  const int c = l & 15, q = l >> 4;
  const bool special = (k == 0) || (k == 224);
  const int kb = k == 224 ? 0 : k;
  bf8 z[2][2][2];
  {
    const unsigned o = WS_X1 + kb * 8192 + c * 256 + (q >> 1) * 64 + (q & 1) * 16 + (k == 224 ? 32 : 0);
#pragma unroll
    for (int u = 0; u < 2; u++)
#pragma unroll
      for (int hf = 0; hf < 2; hf++) {
        z[u][0][hf] = asbf8(__builtin_amdgcn_raw_buffer_load_b128(rws, o + u * 4096 + hf * 128, 0, AUX));
        u4 t = __builtin_amdgcn_raw_buffer_load_b128(rws, (o | 32) + u * 4096 + hf * 128, 0, AUX);
        z[u][1][hf] = asbf8(special ? u4{0u, 0u, 0u, 0u} : t);
      }
  }
  u4 wt[2];
#pragma unroll
  for (int u = 0; u < 2; u++) wt[u] = __builtin_amdgcn_raw_buffer_load_b128(rws, WS_WT + k * 8192 + (w4 * 2 + u) * 1024 + l * 16, 0, AUX);
  bf8 C64[2], S64[2], N64[2], C32[2], S32[2], N32[2];
  const float f1 = (float)(16 * w4 + c);
#pragma unroll
  for (int e = 0; e < 2; e++) {
    float d = f1 * (1.0f / 64.0f);
    float a0 = d * (float)(32 * e + 16 * (q >> 1) + 4 * (q & 1));
    C64[e] = gen8(a0, d, 8.f * d); S64[e] = gen8(a0 - 0.25f, d, 8.f * d); N64[e] = neg8(S64[e]);
    float d2 = (float)(16 * e + c) * (1.0f / 32.0f);
    float b0 = d2 * (float)(4 * q);
    C32[e] = gen8(b0, d2, 16.f * d2); S32[e] = gen8(b0 - 0.25f, d2, 16.f * d2); N32[e] = neg8(S32[e]);
  }
  float tc[2][4], ts[2][4], ic[2][4], is[2][4];
  const float scale = (special ? 1.0f : 2.0f) / (2048.0f * 448.0f);
#pragma unroll
  for (int u = 0; u < 2; u++)
#pragma unroll
    for (int i = 0; i < 4; i++) {
      float a = (float)(16 * u + 4 * q + i) * f1 * (1.0f / 2048.0f);
      tc[u][i] = cosr(a); ts[u][i] = sinr(a);
      float b = (float)((16 * u + c) * (16 * w4 + 4 * q + i)) * (1.0f / 2048.0f);
      ic[u][i] = cosr(b) * scale; is[u][i] = sinr(b) * scale;
    }
  const f4 zero = {0.f, 0.f, 0.f, 0.f};
  f4 re[2], im[2];
#pragma unroll
  for (int u = 0; u < 2; u++) {
    f4 a = mfma16(z[u][0][0], C64[0], zero), b = mfma16(z[u][1][0], C64[0], zero);
    a = mfma16(z[u][0][1], C64[1], a); b = mfma16(z[u][1][1], C64[1], b);
    a = mfma16(z[u][1][0], S64[0], a); b = mfma16(z[u][0][0], N64[0], b);
    a = mfma16(z[u][1][1], S64[1], a); b = mfma16(z[u][0][1], N64[1], b);
#pragma unroll
    for (int i = 0; i < 4; i++) { re[u][i] = a[i] * tc[u][i] + b[i] * ts[u][i]; im[u][i] = b[i] * tc[u][i] - a[i] * ts[u][i]; }
  }
  bf8 pr = pack44(re[0], re[1]), pi = pack44(im[0], im[1]);
#pragma unroll
  for (int u = 0; u < 2; u++) {
    f4 a = mfma16(C32[u], pr, zero), b = mfma16(C32[u], pi, zero);
    a = mfma16(S32[u], pi, a); b = mfma16(N32[u], pr, b);
#pragma unroll
    for (int i = 0; i < 4; i++) { float wr = bflo(wt[u][i]), wi = bfhi(wt[u][i]); re[u][i] = a[i] * wr - b[i] * wi; im[u][i] = a[i] * wi + b[i] * wr; }
  }
  pr = pack44(re[0], re[1]); pi = pack44(im[0], im[1]);
  {
    const unsigned wo = lds + (w4 >> 1) * 1024 + ((2 * (w4 & 1) + (q & 1)) * 16 + c) * 16 + (q >> 1) * 8;
#pragma unroll
    for (int u = 0; u < 2; u++) {
      f4 a = mfma16(pr, C32[u], zero), b = mfma16(pi, C32[u], zero);
      a = mfma16(pi, N32[u], a); b = mfma16(pr, S32[u], b);
      float xr[4], xi[4];
#pragma unroll
      for (int i = 0; i < 4; i++) { xr[i] = a[i] * ic[u][i] - b[i] * is[u][i]; xi[i] = b[i] * ic[u][i] + a[i] * is[u][i]; }
      *(LAS u2*)(uintptr_t)(wo + (u * 2 + 0) * 2048) = u2{pk(xr[0], xr[1]), pk(xr[2], xr[3])};
      *(LAS u2*)(uintptr_t)(wo + (u * 2 + 1) * 2048) = u2{pk(xi[0], xi[1]), pk(xi[2], xi[3])};
    }
  }
  __syncthreads();
#pragma unroll
  for (int u = 0; u < 2; u++) {
    bf8 ur0 = lds128(lds + (u * 2 + 0) * 2048 + l * 16), ur1 = lds128(lds + (u * 2 + 0) * 2048 + 1024 + l * 16);
    bf8 ui0 = lds128(lds + (u * 2 + 1) * 2048 + l * 16), ui1 = lds128(lds + (u * 2 + 1) * 2048 + 1024 + l * 16);
    f4 a = mfma16(C64[0], ur0, zero), b = mfma16(C64[0], ui0, zero);
    a = mfma16(C64[1], ur1, a); b = mfma16(C64[1], ui1, b);
    a = mfma16(N64[0], ui0, a); b = mfma16(S64[0], ur0, b);
    a = mfma16(N64[1], ui1, a); b = mfma16(S64[1], ur1, b);
    const unsigned o = WS_Y1 + kb * 8192 + (16 * u + c) * 256 + w4 * 64 + q * 16;
    u4 v = asu4(pack44(a, b));
    if (!special) __builtin_amdgcn_raw_buffer_store_b128(v, rws, o, 0, AUX);
    else __builtin_amdgcn_raw_buffer_store_b64(u2{v[0], v[1]}, rws, o + (k == 224 ? 8 : 0), 0, AUX);
  }
}

__global__ __launch_bounds__(256) void k_s2(Params p) {
  extern __shared__ __attribute__((aligned(16))) char smem[];
  STAMP(0, 8);
  fft_col(p, blockIdx.x, __builtin_amdgcn_readfirstlane(threadIdx.x >> 6), threadIdx.x & 63, 0);
  WAITV(0);
  STAMP(0, 9);
}

__global__ __launch_bounds__(512, 2) void k_s3(Params p) {
  extern __shared__ __attribute__((aligned(16))) char smem[];
  STAMP(0, 12);
  const int b = blockIdx.x, xc = b & 7, i = b >> 3;
  const int l = threadIdx.x & 63, w = __builtin_amdgcn_readfirstlane(threadIdx.x >> 6), h = l >> 5;
  const int s2 = xc * 4 + i / 7, ct = i % 7;
  const rsrc_t rws = __builtin_amdgcn_make_buffer_rsrc(p.ws, 0, WS_END, 0x00020000);
  const unsigned w2off = (224 * w + (l >> 3)) * 896 + ct * 128 + (((l & 7) ^ (((l >> 4) & 1) << 2)) << 4);
  const unsigned mine = w * 20480;
#pragma unroll
  for (int pc = 0; pc < 5; pc++)
#pragma unroll
    for (int j = 0; j < 4; j++) glds16(w2off, p.w2 + (pc * 32 + j * 8) * 896, mine + pc * 4096 + j * 1024);
  const int q = (l >> 2) & 3, pp = l & 3, dh = (l >> 4) & 1;
  unsigned wa[2];
#pragma unroll
  for (int T = 0; T < 2; T++) wa[T] = mine + (4 * h + q) * 128 + (((4 * T + 2 * dh + (pp >> 1)) ^ ((q >> 1) << 2)) << 4) + (pp & 1) * 8;
  f16v acc[2][2];
#pragma unroll
  for (int a = 0; a < 2; a++)
#pragma unroll
    for (int c = 0; c < 2; c++)
#pragma unroll
      for (int e = 0; e < 16; e++) acc[a][c][e] = 0.f;
  const unsigned ho = WS_H + (s2 * 112 + 14 * w) * 2048 + l * 16;
  bf8 hf[14][2];
#pragma unroll
  for (int st = 0; st < 14; st++)
#pragma unroll
    for (int T = 0; T < 2; T++) {
      u4 t; asm volatile("buffer_load_dwordx4 %0, %1, %2, 0 offen offset:%3" : "=v"(t) : "v"(ho + (st >> 1) * 4096), "s"(rws), "i"((st & 1) * 2048 + T * 1024) : "memory");
      hf[st][T] = asbf8(t);
    }
  bf8 wf[2][2][2];
  asm volatile("s_waitcnt vmcnt(40)" ::: "memory");
  bf8 w01[4][2];
#pragma unroll
  for (int st = 0; st < 4; st++)
#pragma unroll
    for (int T = 0; T < 2; T++) w01[st][T] = cat(ldstr(wa[T] + st * 2048), ldstr(wa[T] + st * 2048 + 1024));
  WAITL0();
#pragma unroll
  for (int pc = 5; pc < 7; pc++)
#pragma unroll
    for (int j = 0; j < 4; j++) glds16(w2off, p.w2 + (pc * 32 + j * 8) * 896, mine + (pc - 5) * 4096 + j * 1024);
  const int ns = w < 4 ? 4 : 3, st0 = w < 4 ? 4 * w : 16 + 3 * (w - 4);
  u4 y[4][2];
#pragma unroll
  for (int e = 0; e < 4; e++)
#pragma unroll
    for (int j = 0; j < 2; j++) {
      int col = 8 * (st0 + (e < ns ? e : 0)) + 4 * j + (l >> 4);
      asm volatile("buffer_load_dwordx4 %0, %1, %2, 0 offen" : "=v"(y[e][j]) : "v"(WS_Y1 + col * 8192 + s2 * 256 + (l & 15) * 16), "s"(rws) : "memory");
    }
#pragma unroll
  for (int st = 0; st < 14; st++) {
    if (st < 10) asm volatile("s_waitcnt vmcnt(%2)" : "+v"(hf[st][0]), "+v"(hf[st][1]) : "i"(16 + 26 - 2 * st) : "memory");
    else asm volatile("s_waitcnt vmcnt(%2)" : "+v"(hf[st][0]), "+v"(hf[st][1]) : "i"(st < 12 ? 12 : 8) : "memory");
    bf8 a[2];
#pragma unroll
    for (int T = 0; T < 2; T++) {
      if (st < 4) a[T] = w01[st][T];
      else { const int o = (st < 10 ? st : st - 10) * 2048; a[T] = cat(ldstr(wa[T] + o), ldstr(wa[T] + o + 1024)); }
    }
#pragma unroll
    for (int Td = 0; Td < 2; Td++)
#pragma unroll
      for (int Ts = 0; Ts < 2; Ts++) acc[Td][Ts] = mfma32(a[Td], hf[st][Ts], acc[Td][Ts]);
  }
  STAMP(0, 13);
  asm volatile("s_waitcnt vmcnt(0)" : "+v"(y[0][0]), "+v"(y[0][1]), "+v"(y[1][0]), "+v"(y[1][1]), "+v"(y[2][0]), "+v"(y[2][1]), "+v"(y[3][0]), "+v"(y[3][1]) :: "memory");
  const unsigned ys = mine + 8192;
#pragma unroll
  for (int e = 0; e < 4; e++)
#pragma unroll
    for (int j = 0; j < 2; j++) *(LAS u4*)(uintptr_t)(ys + e * 2048 + (4 * j + (l >> 4)) * 256 + (((l & 15) ^ (((l >> 4) & 1) << 3)) << 4)) = y[e][j];
#pragma unroll
  for (int e = 0; e < 4; e++) {
    if (e < ns) {
      bf8 yb[2], ga[2];
#pragma unroll
      for (int T = 0; T < 2; T++) {
        const unsigned a = ys + e * 2048 + (4 * h + (q >> 1)) * 256 + (((8 * T + 4 * dh + pp) ^ ((q >> 1) << 3)) << 4) + (q & 1) * 8;
        yb[T] = cat(ldstr(a), ldstr(a + 512));
        const int d = 64 * ct + 32 * T + (l & 31), k0 = 8 * (st0 + e) + 4 * h;
        const float a0 = (float)((k0 * d) % 448) * (1.0f / 448.0f), dd = (float)d * (1.0f / 448.0f);
        float v[8];
#pragma unroll
        for (int j = 0; j < 8; j++) v[j] = cosr(a0 + (float)(j >> 1) * dd + ((j & 1) ? 0.25f : 0.f));
        if (k0 == 0) v[1] = (d & 1) ? -1.f : 1.f;
        ga[T] = asbf8(u4{pk(v[0], v[1]), pk(v[2], v[3]), pk(v[4], v[5]), pk(v[6], v[7])});
      }
#pragma unroll
      for (int Td = 0; Td < 2; Td++)
#pragma unroll
        for (int Ts = 0; Ts < 2; Ts++) acc[Td][Ts] = mfma32(ga[Td], yb[Ts], acc[Td][Ts]);
    }
  }
  const int row = 8 * w + (l >> 3), dg = l & 7;
  const size_t go = (size_t)(32 * row + s2) * 896 + ct * 128 + dg * 16;
  const u4 xr = *(const u4*)(p.x + go);
  const u4 bb = *(const u4*)(p.b2 + ct * 128 + dg * 16);
  WAITL0();
  BAR();
  STAMP(0, 14);
#pragma unroll
  for (int Td = 0; Td < 2; Td++)
#pragma unroll
    for (int Ts = 0; Ts < 2; Ts++)
#pragma unroll
      for (int g = 0; g < 4; g++) {
        const f16v& a = acc[Td][Ts];
        *(LAS f4*)(uintptr_t)(w * 16384 + (32 * Ts + (l & 31)) * 256 + (((8 * Td + 2 * g + h) ^ (l & 7)) << 4)) = f4{a[4 * g], a[4 * g + 1], a[4 * g + 2], a[4 * g + 3]};
      }
  WAITL0();
  BAR();
  f4 s0 = {bflo(xr[0]) + bflo(bb[0]), bfhi(xr[0]) + bfhi(bb[0]), bflo(xr[1]) + bflo(bb[1]), bfhi(xr[1]) + bfhi(bb[1])};
  f4 s1 = {bflo(xr[2]) + bflo(bb[2]), bfhi(xr[2]) + bfhi(bb[2]), bflo(xr[3]) + bflo(bb[3]), bfhi(xr[3]) + bfhi(bb[3])};
#pragma unroll
  for (int v = 0; v < 8; v++) {
    s0 += *(const LAS f4*)(uintptr_t)(v * 16384 + row * 256 + (((2 * dg) ^ (row & 7)) << 4));
    s1 += *(const LAS f4*)(uintptr_t)(v * 16384 + row * 256 + (((2 * dg + 1) ^ (row & 7)) << 4));
  }
  *(u4*)(p.out + go) = asu4(pack44(s0, s1));
  WAITV(0);
  STAMP(0, 15);
}

extern "C" void kernel_launch(void* const* d_in, const int* in_sizes, int n_in, void* d_out, int out_size, void* d_ws, size_t ws_size, hipStream_t stream) {
  Params p;
  p.x = (const char*)d_in[0]; p.wr = (const char*)d_in[1]; p.wi = (const char*)d_in[2]; p.w1 = (const char*)d_in[4];
  p.b1 = (const char*)d_in[5]; p.w2 = (const char*)d_in[6]; p.b2 = (const char*)d_in[7]; p.out = (char*)d_out; p.ws = (char*)d_ws;
  (void)hipFuncSetAttribute((const void*)k_s1, hipFuncAttributeMaxDynamicSharedMemorySize, L_TOTAL);
  (void)hipFuncSetAttribute((const void*)k_s3, hipFuncAttributeMaxDynamicSharedMemorySize, L_TOTAL);
  k_s1<<<256, 512, L_TOTAL, stream>>>(p);
  k_s2<<<225, 256, 8192, stream>>>(p);
  k_s3<<<224, 512, L_TOTAL, stream>>>(p);
}
```

```cpp
#define METRIC 0
#include <hip/hip_runtime.h>
#include <stdint.h>

typedef __bf16 bf2 __attribute__((ext_vector_type(2)));
typedef __bf16 bf4 __attribute__((ext_vector_type(4)));
typedef __bf16 bf8 __attribute__((ext_vector_type(8)));
typedef float f2 __attribute__((ext_vector_type(2)));
typedef float f4 __attribute__((ext_vector_type(4)));
typedef float f16v __attribute__((ext_vector_type(16)));
typedef unsigned u2 __attribute__((ext_vector_type(2)));
typedef unsigned u4 __attribute__((ext_vector_type(4)));
typedef unsigned long long u64;
#define LAS __attribute__((address_space(3)))
#define DI __device__ __forceinline__
typedef __amdgpu_buffer_rsrc_t rsrc_t;

constexpr int WS_WT = 65536;
constexpr int WS_X1 = WS_WT + 232 * 8192;
constexpr int WS_Y1 = WS_X1 + 224 * 8192;
constexpr int WS_H = WS_Y1 + 224 * 8192;
constexpr int WS_END = WS_H + 32 * 112 * 2048;
constexpr int AUX = 0;

#define STAMP(w, i)

struct Params {
  const char* x; const char* wr; const char* wi; const char* w1; const char* b1; const char* w2; const char* b2;
  char* out; char* ws;
};

DI unsigned pk(float a, float b) { f2 v = {a, b}; bf2 r = __builtin_convertvector(v, bf2); return __builtin_bit_cast(unsigned, r); }
DI float rbf(float a) { return __uint_as_float(pk(a, a) << 16); }
DI float bflo(unsigned u) { return __uint_as_float(u << 16); }
DI float bfhi(unsigned u) { return __uint_as_float(u & 0xffff0000u); }
DI bf8 asbf8(u4 v) { return __builtin_bit_cast(bf8, v); }
DI u4 asu4(bf8 v) { return __builtin_bit_cast(u4, v); }
DI float dot2(unsigned a, unsigned b, float c) { return __builtin_amdgcn_fdot2_f32_bf16(__builtin_bit_cast(bf2, a), __builtin_bit_cast(bf2, b), c, false); }
DI f16v mfma32(bf8 a, bf8 b, f16v c) { return __builtin_amdgcn_mfma_f32_32x32x16_bf16(a, b, c, 0, 0, 0); }
DI f4 mfma16(bf8 a, bf8 b, f4 c) { return __builtin_amdgcn_mfma_f32_16x16x32_bf16(a, b, c, 0, 0, 0); }
DI bf8 lds128(unsigned a) { return *(const LAS bf8*)(uintptr_t)a; }
DI bf4 ldstr(unsigned a) { return __builtin_amdgcn_ds_read_tr16_b64_v4bf16((LAS bf4*)(uintptr_t)a); }
DI bf8 cat(bf4 a, bf4 b) { return __builtin_shufflevector(a, b, 0, 1, 2, 3, 4, 5, 6, 7); }
DI float xhalf(float v) { unsigned u = __float_as_uint(v); u2 r = __builtin_amdgcn_permlane32_swap(u, u, false, false); unsigned a = r[0], b = r[1]; return __uint_as_float(a) + __uint_as_float(b); }
DI float cosr(float rev) { return __builtin_amdgcn_cosf(rev); }
DI float sinr(float rev) { return __builtin_amdgcn_sinf(rev); }
DI void glds16(unsigned voff, const char* sbase, unsigned lds) {
  unsigned keep;
  asm volatile("s_mov_b32 %0, m0\n\ts_mov_b32 m0, %3\n\ts_nop 0\n\tglobal_load_lds_dwordx4 %1, %2\n\ts_mov_b32 m0, %0" : "=&s"(keep) : "v"(voff), "s"(sbase), "s"(lds) : "memory");
}
DI void glds4(unsigned voff, const char* sbase, unsigned lds) {
  unsigned keep;
  asm volatile("s_mov_b32 %0, m0\n\ts_mov_b32 m0, %3\n\ts_nop 0\n\tglobal_load_lds_dword %1, %2\n\ts_mov_b32 m0, %0" : "=&s"(keep) : "v"(voff), "s"(sbase), "s"(lds) : "memory");
}
#define WAITV(n) asm volatile("s_waitcnt vmcnt(" #n ")" ::: "memory")
#define WAITL0() asm volatile("s_waitcnt lgkmcnt(0)" ::: "memory")
#define BAR() do { asm volatile("" ::: "memory"); __builtin_amdgcn_s_barrier(); asm volatile("" ::: "memory"); } while (0)
#define SCHED() __builtin_amdgcn_sched_barrier(0)

constexpr unsigned L_DFT = 131072;
constexpr unsigned L_B1 = L_DFT + 28672;
constexpr unsigned L_TOTAL = 163840;

DI u4 gelu8(const f16v& a, int o, float r) {
  float g[8];
#pragma unroll
  for (int i = 0; i < 8; i++) {
    float u = a[o + i] * r;
    float z = u * __builtin_fmaf(u * u, -0.10294324f, -2.3022082f);
    g[i] = u * __builtin_amdgcn_rcpf(1.0f + __builtin_amdgcn_exp2f(z));
  }
  return u4{pk(g[0], g[1]), pk(g[2], g[3]), pk(g[4], g[5]), pk(g[6], g[7])};
}
DI bf8 split3(float c, unsigned bias) {
  float hi = rbf(c), mid = rbf(c - hi), lo = c - hi - mid;
  return asbf8(u4{pk(hi, mid), (pk(lo, lo) & 0xffffu) | (bias << 16), 0u, 0u});
}

template <int G> struct Frag { bf8 x; bf8 w[G == 0 ? 3 : 2]; };

template <int G>
DI void s1_body(const Params& p, int rt, int ft, int s2o, int cto, int w, int l) {
  const int sg = w & 3, h = l >> 5;
  unsigned xoff[2], woff[2];
#pragma unroll
  for (int j = 0; j < 2; j++) {
    int row = 16 * w + 8 * j + (l >> 3);
    xoff[j] = (32 * (row & 63) + 16 * (row >> 6) + rt) * 896 + (((l & 7) ^ ((row >> 1) & 7)) << 4);
    int kr = 8 * w + 4 * j + (l >> 4);
    woff[j] = kr * 3584 + ft * 256 + (((l & 15) ^ ((kr & 3) << 2)) << 4);
  }
  const unsigned w2off = (224 * w + (l >> 3)) * 896 + cto * 128 + (((l & 7) ^ (((l >> 4) & 1) << 2)) << 4);
  if (w == 0) glds4(l * 4, p.b1 + ft * 256, L_B1);
#pragma unroll
  for (int c = 0; c < 4; c++) {
#pragma unroll
    for (int j = 0; j < 2; j++) glds16(xoff[j], p.x + c * 128, c * 32768 + (2 * w + j) * 1024);
#pragma unroll
    for (int j = 0; j < 2; j++) glds16(woff[j], p.w1 + c * 64 * 3584, c * 32768 + 16384 + (2 * w + j) * 1024);
  }
  {
    int c = l & 31, k = 16 * ft + (c & 15);
    float ph = (c >> 4) ? 0.25f : 0.0f;
    if (k == 0 && c == 16) { k = 224; ph = 0.f; }
    const float dk = (float)k * (1.0f / 448.0f);
    for (int st = w; st < 28; st += 8) {
      int b0 = ((16 * st + 8 * h) * k) % 448;
      float a0 = __builtin_fmaf((float)b0, 1.0f / 448.0f, ph);
      float v[8];
#pragma unroll
      for (int j = 0; j < 8; j++) v[j] = cosr(__builtin_fmaf((float)j, dk, a0));
      *(LAS u4*)(uintptr_t)(L_DFT + st * 1024 + l * 16) = u4{pk(v[0], v[1]), pk(v[2], v[3]), pk(v[4], v[5]), pk(v[6], v[7])};
    }
  }
  unsigned xa[4], wa[G == 0 ? 3 : 1];
  {
    int row = 32 * sg + (l & 31);
#pragma unroll
    for (int v = 0; v < 4; v++) xa[v] = row * 128 + (((2 * v + h) ^ ((row >> 1) & 7)) << 4);
    int q = (l >> 2) & 3, pp = l & 3, fh = (l >> 4) & 1;
#pragma unroll
    for (int t = 0; t < (G == 0 ? 3 : 1); t++) {
      int T = G == 0 ? t : 3;
      wa[t] = 16384 + (8 * h + q) * 256 + (((4 * T + 2 * fh + (pp >> 1)) ^ (q << 2)) << 4) + (pp & 1) * 8;
    }
  }
  f16v acc[G == 0 ? 3 : 2];
#pragma unroll
  for (int t = 0; t < (G == 0 ? 3 : 2); t++)
#pragma unroll
    for (int i = 0; i < 16; i++) acc[t][i] = 0.f;
  float sx0 = 0.f, sx1 = 0.f, sq0 = 0.f, sq1 = 0.f, cs[G == 0 ? 3 : 2];
#pragma unroll
  for (int t = 0; t < (G == 0 ? 3 : 2); t++) cs[t] = 0.f;

  auto load = [&](Frag<G>& f, unsigned base, unsigned dbase, int v) {
    f.x = lds128(base + xa[v]);
#pragma unroll
    for (int t = 0; t < (G == 0 ? 3 : 1); t++) f.w[t] = cat(ldstr(base + wa[t] + v * 4096), ldstr(base + wa[t] + v * 4096 + 1024));
    if (G == 1) f.w[1] = lds128(dbase + v * 1024);
  };
  auto mma = [&](const Frag<G>& f, int v) {
    if (G == 0) {
#pragma unroll
      for (int t = 0; t < 3; t++) acc[t] = mfma32(f.w[t], f.x, acc[t]);
    } else {
      acc[0] = mfma32(f.w[0], f.x, acc[0]);
      acc[1] = mfma32(f.x, f.w[1], acc[1]);
    }
    u4 xu = asu4(f.x);
    sx0 = dot2(xu[0], 0x3f803f80u, sx0); sq0 = dot2(xu[0], xu[0], sq0);
    sx1 = dot2(xu[1], 0x3f803f80u, sx1); sq1 = dot2(xu[1], xu[1], sq1);
    sx0 = dot2(xu[2], 0x3f803f80u, sx0); sq0 = dot2(xu[2], xu[2], sq0);
    sx1 = dot2(xu[3], 0x3f803f80u, sx1); sq1 = dot2(xu[3], xu[3], sq1);
    if (v == sg) {
#pragma unroll
      for (int i = 0; i < 4; i++)
#pragma unroll
        for (int t = 0; t < (G == 0 ? 3 : 1); t++) cs[t] = dot2(asu4(f.w[t])[i], 0x3f803f80u, cs[t]);
    }
    if (G == 1) {
#pragma unroll
      for (int i = 0; i < 4; i++) cs[1] = dot2(asu4(f.w[1])[i], 0x3f803f80u, cs[1]);
    }
  };
  STAMP(0, 1);
  asm volatile("s_waitcnt vmcnt(12) lgkmcnt(0)" ::: "memory");
  BAR();
  STAMP(0, 2);
  Frag<G> fa, fb;
  load(fa, 0, L_DFT + l * 16, 0);
#pragma unroll 1
  for (int c = 0; c < 7; c++) {
    const unsigned base = (c & 3) * 32768, db = L_DFT + l * 16 + c * 4096;
    load(fb, base, db, 1); mma(fa, 0); SCHED();
    load(fa, base, db, 2); mma(fb, 1); SCHED();
    load(fb, base, db, 3); mma(fa, 2); SCHED();
    if (c < 6) asm volatile("s_waitcnt vmcnt(8) lgkmcnt(0)" ::: "memory"); else WAITL0();
    BAR();
    if (c < 3) {
#pragma unroll
      for (int j = 0; j < 2; j++) glds16(xoff[j], p.x + (c + 4) * 128, base + (2 * w + j) * 1024);
#pragma unroll
      for (int j = 0; j < 2; j++) glds16(woff[j], p.w1 + (c + 4) * 64 * 3584, base + 16384 + (2 * w + j) * 1024);
    } else {
#pragma unroll
      for (int j = 0; j < 4; j++) glds16(w2off, p.w2 + ((c - 3) * 32 + j * 8) * 896, base + w * 4096 + j * 1024);
    }
    if (c < 6) load(fa, ((c + 1) & 3) * 32768, db + 4096, 0);
    mma(fb, 3); SCHED();
  }
  STAMP(0, 3);
#pragma unroll
  for (int t = 0; t < (G == 0 ? 3 : 1); t++) *(LAS float*)(uintptr_t)(L_DFT + ((sg * 4 + (G == 0 ? t : 3)) * 64 + l) * 4) = cs[t];
  const float sum = xhalf(sx0 + sx1), ssq = xhalf(sq0 + sq1);
  const float mean = sum * (1.0f / 448.0f);
  const float var = __builtin_fmaxf(__builtin_fmaf(-mean, mean, ssq * (1.0f / 448.0f)), 0.f);
  const float sd = rbf(__builtin_sqrtf(rbf(rbf(var) + 1e-5f)));
  const float r = 1.0f / sd;
  const bf8 ext = asbf8(u4{pk(-mean, -mean), pk(-mean, sd), 0u, 0u});
  if (G == 1 && h == 0) *(LAS float*)(uintptr_t)(L_DFT + 4096 + sg * 128 + l * 4) = r;
  WAITL0();
  BAR();
  const rsrc_t rws = __builtin_amdgcn_make_buffer_rsrc(p.ws, 0, WS_END, 0x00020000);
  const int s2 = 16 * (sg >> 1) + rt;
  if (G == 1) {
    acc[1] = mfma32(ext, split3(cs[1], 0u), acc[1]);
    const int c = l & 31;
    const unsigned o = WS_X1 + (16 * ft + (c & 15)) * 8192 + s2 * 256 + (sg & 1) * 128 + (c >> 4) * 32 + h * 16;
#pragma unroll
    for (int t = 0; t < 2; t++) {
      f4 r0 = *(const LAS f4*)(uintptr_t)(L_DFT + 4096 + sg * 128 + (16 * t + 4 * h) * 4);
      f4 r1 = *(const LAS f4*)(uintptr_t)(L_DFT + 4096 + sg * 128 + (16 * t + 8 + 4 * h) * 4);
      const f16v& a = acc[1];
      u4 v = {pk(a[8 * t] * r0[0], a[8 * t + 1] * r0[1]), pk(a[8 * t + 2] * r0[2], a[8 * t + 3] * r0[3]),
              pk(a[8 * t + 4] * r1[0], a[8 * t + 5] * r1[1]), pk(a[8 * t + 6] * r1[2], a[8 * t + 7] * r1[3])};
      __builtin_amdgcn_raw_buffer_store_b128(v, rws, o + t * 64, 0, AUX);
    }
  }
#pragma unroll
  for (int t = 0; t < (G == 0 ? 3 : 1); t++) {
    const int T = G == 0 ? t : 3;
    float c = 0.f;
#pragma unroll
    for (int s = 0; s < 4; s++) c += *(const LAS float*)(uintptr_t)(L_DFT + ((s * 4 + T) * 64 + l) * 4);
    unsigned bias = *(const LAS unsigned short*)(uintptr_t)(L_B1 + (T * 32 + (l & 31)) * 2);
    acc[t] = mfma32(split3(c, h ? 0u : bias), ext, acc[t]);
  }
#pragma unroll
  for (int t = 0; t < (G == 0 ? 3 : 1); t++) {
    const int T = G == 0 ? t : 3;
    const unsigned o = WS_H + (((s2 * 112 + ft * 8 + T * 2) * 2 + (sg & 1)) * 64 + l) * 16;
    __builtin_amdgcn_raw_buffer_store_b128(gelu8(acc[t], 0, r), rws, o, 0, AUX);
    __builtin_amdgcn_raw_buffer_store_b128(gelu8(acc[t], 8, r), rws, o + 2048, 0, AUX);
  }
  STAMP(0, 4);
}

DI void helper_wt(const Params& p, int id) {
  const int tid = threadIdx.x;
  const int w4 = id >> 3, f20 = (id & 7) * 4;
  for (int e = tid; e < 3600; e += 512) {
    int seg = e / 450, o = e - seg * 450;
    const char* src = (seg >= 4 ? p.wi : p.wr) + (size_t)(16 * w4 + 64 * (f20 + (seg & 3))) * 450 + o * 16;
    *(LAS u4*)(uintptr_t)(e * 16) = *(const u4*)src;
  }
  __syncthreads();
  const rsrc_t rws = __builtin_amdgcn_make_buffer_rsrc(p.ws, 0, WS_END, 0x00020000);
  for (int e = tid; e < 3600; e += 512) {
    int k = e >> 4, c = e & 15;
    u4 v;
#pragma unroll
    for (int i = 0; i < 4; i++) {
      unsigned a = *(const LAS unsigned short*)(uintptr_t)(i * 7200 + c * 450 + k * 2);
      unsigned b = *(const LAS unsigned short*)(uintptr_t)(28800 + i * 7200 + c * 450 + k * 2);
      v[i] = a | (b << 16);
    }
    __builtin_amdgcn_raw_buffer_store_b128(v, rws, WS_WT + k * 8192 + id * 256 + c * 16, 0, AUX);
  }
}

__global__ __launch_bounds__(512, 2) void k_s1(Params p) {
  extern __shared__ __attribute__((aligned(16))) char smem[];
  STAMP(0, 0);
  const int b = blockIdx.x, xc = b & 7, i = b >> 3;
  const int l = threadIdx.x & 63, w = __builtin_amdgcn_readfirstlane(threadIdx.x >> 6);
  if (i >= 28) { helper_wt(p, xc * 4 + (i - 28)); return; }
  const int rt = (xc >> 1) * 4 + i / 7, ft = (xc & 1) * 7 + i % 7;
  const int s2o = xc * 4 + i / 7, cto = i % 7;
  if (w < 4) s1_body<0>(p, rt, ft, s2o, cto, w, l); else s1_body<1>(p, rt, ft, s2o, cto, w, l);
  WAITV(0);
  STAMP(0, 5);
}

DI bf8 gen8(float a0, float da_lo, float da_hi) {
  float v[8];
#pragma unroll
  for (int j = 0; j < 8; j++) v[j] = cosr(a0 + (float)(j & 3) * da_lo + (float)(j >> 2) * da_hi);
  return asbf8(u4{pk(v[0], v[1]), pk(v[2], v[3]), pk(v[4], v[5]), pk(v[6], v[7])});
}
DI bf8 neg8(bf8 a) { u4 v = asu4(a); return asbf8(v ^ 0x80008000u); }
DI bf8 pack44(f4 a, f4 b) { return asbf8(u4{pk(a[0], a[1]), pk(a[2], a[3]), pk(b[0], b[1]), pk(b[2], b[3])}); }

DI void fft_col(const Params& p, int k, int w4, int l, unsigned lds) {
  const rsrc_t rws = __builtin_amdgcn_make_buffer_rsrc(p.ws, 0, WS_END, 0x00020000);
  const int c = l & 15, q = l >> 4;
  const bool special = (k == 0) || (k == 224);
  const int kb = k == 224 ? 0 : k;
  bf8 z[2][2][2];
  {
    const unsigned o = WS_X1 + kb * 8192 + c * 256 + (q >> 1) * 64 + (q & 1) * 16 + (k == 224 ? 32 : 0);
#pragma unroll
    for (int u = 0; u < 2; u++)
#pragma unroll
      for (int hf = 0; hf < 2; hf++) {
        z[u][0][hf] = asbf8(__builtin_amdgcn_raw_buffer_load_b128(rws, o + u * 4096 + hf * 128, 0, AUX));
        u4 t = __builtin_amdgcn_raw_buffer_load_b128(rws, (o | 32) + u * 4096 + hf * 128, 0, AUX);
        z[u][1][hf] = asbf8(special ? u4{0u, 0u, 0u, 0u} : t);
      }
  }
  u4 wt[2];
#pragma unroll
  for (int u = 0; u < 2; u++) wt[u] = __builtin_amdgcn_raw_buffer_load_b128(rws, WS_WT + k * 8192 + (w4 * 2 + u) * 1024 + l * 16, 0, AUX);
  bf8 C64[2], S64[2], N64[2], C32[2], S32[2], N32[2];
  const float f1 = (float)(16 * w4 + c);
#pragma unroll
  for (int e = 0; e < 2; e++) {
    float d = f1 * (1.0f / 64.0f);
    float a0 = d * (float)(32 * e + 16 * (q >> 1) + 4 * (q & 1));
    C64[e] = gen8(a0, d, 8.f * d); S64[e] = gen8(a0 - 0.25f, d, 8.f * d); N64[e] = neg8(S64[e]);
    float d2 = (float)(16 * e + c) * (1.0f / 32.0f);
    float b0 = d2 * (float)(4 * q);
    C32[e] = gen8(b0, d2, 16.f * d2); S32[e] = gen8(b0 - 0.25f, d2, 16.f * d2); N32[e] = neg8(S32[e]);
  }
  float tc[2][4], ts[2][4], ic[2][4], is[2][4];
  const float scale = (special ? 1.0f : 2.0f) / (2048.0f * 448.0f);
#pragma unroll
  for (int u = 0; u < 2; u++)
#pragma unroll
    for (int i = 0; i < 4; i++) {
      float a = (float)(16 * u + 4 * q + i) * f1 * (1.0f / 2048.0f);
      tc[u][i] = cosr(a); ts[u][i] = sinr(a);
      float b = (float)((16 * u + c) * (16 * w4 + 4 * q + i)) * (1.0f / 2048.0f);
      ic[u][i] = cosr(b) * scale; is[u][i] = sinr(b) * scale;
    }
  const f4 zero = {0.f, 0.f, 0.f, 0.f};
  f4 re[2], im[2];
#pragma unroll
  for (int u = 0; u < 2; u++) {
    f4 a = mfma16(z[u][0][0], C64[0], zero), b = mfma16(z[u][1][0], C64[0], zero);
    a = mfma16(z[u][0][1], C64[1], a); b = mfma16(z[u][1][1], C64[1], b);
    a = mfma16(z[u][1][0], S64[0], a); b = mfma16(z[u][0][0], N64[0], b);
    a = mfma16(z[u][1][1], S64[1], a); b = mfma16(z[u][0][1], N64[1], b);
#pragma unroll
    for (int i = 0; i < 4; i++) { re[u][i] = a[i] * tc[u][i] + b[i] * ts[u][i]; im[u][i] = b[i] * tc[u][i] - a[i] * ts[u][i]; }
  }
  bf8 pr = pack44(re[0], re[1]), pi = pack44(im[0], im[1]);
#pragma unroll
  for (int u = 0; u < 2; u++) {
    f4 a = mfma16(C32[u], pr, zero), b = mfma16(C32[u], pi, zero);
    a = mfma16(S32[u], pi, a); b = mfma16(N32[u], pr, b);
#pragma unroll
    for (int i = 0; i < 4; i++) { float wr = bflo(wt[u][i]), wi = bfhi(wt[u][i]); re[u][i] = a[i] * wr - b[i] * wi; im[u][i] = a[i] * wi + b[i] * wr; }
  }
  pr = pack44(re[0], re[1]); pi = pack44(im[0], im[1]);
  {
    const unsigned wo = lds + (w4 >> 1) * 1024 + ((2 * (w4 & 1) + (q & 1)) * 16 + c) * 16 + (q >> 1) * 8;
#pragma unroll
    for (int u = 0; u < 2; u++) {
      f4 a = mfma16(pr, C32[u], zero), b = mfma16(pi, C32[u], zero);
      a = mfma16(pi, N32[u], a); b = mfma16(pr, S32[u], b);
      float xr[4], xi[4];
#pragma unroll
      for (int i = 0; i < 4; i++) { xr[i] = a[i] * ic[u][i] - b[i] * is[u][i]; xi[i] = b[i] * ic[u][i] + a[i] * is[u][i]; }
      *(LAS u2*)(uintptr_t)(wo + (u * 2 + 0) * 2048) = u2{pk(xr[0], xr[1]), pk(xr[2], xr[3])};
      *(LAS u2*)(uintptr_t)(wo + (u * 2 + 1) * 2048) = u2{pk(xi[0], xi[1]), pk(xi[2], xi[3])};
    }
  }
  __syncthreads();
#pragma unroll
  for (int u = 0; u < 2; u++) {
    bf8 ur0 = lds128(lds + (u * 2 + 0) * 2048 + l * 16), ur1 = lds128(lds + (u * 2 + 0) * 2048 + 1024 + l * 16);
    bf8 ui0 = lds128(lds + (u * 2 + 1) * 2048 + l * 16), ui1 = lds128(lds + (u * 2 + 1) * 2048 + 1024 + l * 16);
    f4 a = mfma16(C64[0], ur0, zero), b = mfma16(C64[0], ui0, zero);
    a = mfma16(C64[1], ur1, a); b = mfma16(C64[1], ui1, b);
    a = mfma16(N64[0], ui0, a); b = mfma16(S64[0], ur0, b);
    a = mfma16(N64[1], ui1, a); b = mfma16(S64[1], ur1, b);
    const unsigned o = WS_Y1 + kb * 8192 + (16 * u + c) * 256 + w4 * 64 + q * 16;
    u4 v = asu4(pack44(a, b));
    if (!special) __builtin_amdgcn_raw_buffer_store_b128(v, rws, o, 0, AUX);
    else __builtin_amdgcn_raw_buffer_store_b64(u2{v[0], v[1]}, rws, o + (k == 224 ? 8 : 0), 0, AUX);
  }
}

__global__ __launch_bounds__(256) void k_s2(Params p) {
  extern __shared__ __attribute__((aligned(16))) char smem[];
  STAMP(0, 8);
  fft_col(p, blockIdx.x, __builtin_amdgcn_readfirstlane(threadIdx.x >> 6), threadIdx.x & 63, 0);
  WAITV(0);
  STAMP(0, 9);
}

__global__ __launch_bounds__(512, 2) void k_s3(Params p) {
  extern __shared__ __attribute__((aligned(16))) char smem[];
  STAMP(0, 12);
  const int b = blockIdx.x, xc = b & 7, i = b >> 3;
  const int l = threadIdx.x & 63, w = __builtin_amdgcn_readfirstlane(threadIdx.x >> 6), h = l >> 5;
  const int s2 = xc * 4 + i / 7, ct = i % 7;
  const rsrc_t rws = __builtin_amdgcn_make_buffer_rsrc(p.ws, 0, WS_END, 0x00020000);
  const unsigned w2off = (224 * w + (l >> 3)) * 896 + ct * 128 + (((l & 7) ^ (((l >> 4) & 1) << 2)) << 4);
  const unsigned mine = w * 20480;
#pragma unroll
  for (int pc = 0; pc < 5; pc++)
#pragma unroll
    for (int j = 0; j < 4; j++) glds16(w2off, p.w2 + (pc * 32 + j * 8) * 896, mine + pc * 4096 + j * 1024);
  const int q = (l >> 2) & 3, pp = l & 3, dh = (l >> 4) & 1;
  unsigned wa[2];
#pragma unroll
  for (int T = 0; T < 2; T++) wa[T] = mine + (4 * h + q) * 128 + (((4 * T + 2 * dh + (pp >> 1)) ^ ((q >> 1) << 2)) << 4) + (pp & 1) * 8;
  f16v acc[2][2];
#pragma unroll
  for (int a = 0; a < 2; a++)
#pragma unroll
    for (int c = 0; c < 2; c++)
#pragma unroll
      for (int e = 0; e < 16; e++) acc[a][c][e] = 0.f;
  const unsigned ho = WS_H + (s2 * 112 + 14 * w) * 2048 + l * 16;
  bf8 hf[14][2];
#pragma unroll
  for (int st = 0; st < 14; st++)
#pragma unroll
    for (int T = 0; T < 2; T++) {
      u4 t; asm volatile("buffer_load_dwordx4 %0, %1, %2, 0 offen offset:%3" : "=v"(t) : "v"(ho + (st >> 1) * 4096), "s"(rws), "i"((st & 1) * 2048 + T * 1024) : "memory");
      hf[st][T] = asbf8(t);
    }
  bf8 wf[2][2][2];
  asm volatile("s_waitcnt vmcnt(40)" ::: "memory");
  bf8 w01[4][2];
#pragma unroll
  for (int st = 0; st < 4; st++)
#pragma unroll
    for (int T = 0; T < 2; T++) w01[st][T] = cat(ldstr(wa[T] + st * 2048), ldstr(wa[T] + st * 2048 + 1024));
  WAITL0();
#pragma unroll
  for (int pc = 5; pc < 7; pc++)
#pragma unroll
    for (int j = 0; j < 4; j++) glds16(w2off, p.w2 + (pc * 32 + j * 8) * 896, mine + (pc - 5) * 4096 + j * 1024);
  const int ns = w < 4 ? 4 : 3, st0 = w < 4 ? 4 * w : 16 + 3 * (w - 4);
  u4 y[4][2];
#pragma unroll
  for (int e = 0; e < 4; e++)
#pragma unroll
    for (int j = 0; j < 2; j++) {
      int col = 8 * (st0 + (e < ns ? e : 0)) + 4 * j + (l >> 4);
      asm volatile("buffer_load_dwordx4 %0, %1, %2, 0 offen" : "=v"(y[e][j]) : "v"(WS_Y1 + col * 8192 + s2 * 256 + (l & 15) * 16), "s"(rws) : "memory");
    }
#pragma unroll
  for (int st = 0; st < 14; st++) {
    if (st < 10) asm volatile("s_waitcnt vmcnt(%2)" : "+v"(hf[st][0]), "+v"(hf[st][1]) : "i"(16 + 26 - 2 * st) : "memory");
    else asm volatile("s_waitcnt vmcnt(%2)" : "+v"(hf[st][0]), "+v"(hf[st][1]) : "i"(st < 12 ? 12 : 8) : "memory");
    bf8 a[2];
#pragma unroll
    for (int T = 0; T < 2; T++) {
      if (st < 4) a[T] = w01[st][T];
      else { const int o = (st < 10 ? st : st - 10) * 2048; a[T] = cat(ldstr(wa[T] + o), ldstr(wa[T] + o + 1024)); }
    }
#pragma unroll
    for (int Td = 0; Td < 2; Td++)
#pragma unroll
      for (int Ts = 0; Ts < 2; Ts++) acc[Td][Ts] = mfma32(a[Td], hf[st][Ts], acc[Td][Ts]);
  }
  STAMP(0, 13);
  asm volatile("s_waitcnt vmcnt(0)" : "+v"(y[0][0]), "+v"(y[0][1]), "+v"(y[1][0]), "+v"(y[1][1]), "+v"(y[2][0]), "+v"(y[2][1]), "+v"(y[3][0]), "+v"(y[3][1]) :: "memory");
  const unsigned ys = mine + 8192;
#pragma unroll
  for (int e = 0; e < 4; e++)
#pragma unroll
    for (int j = 0; j < 2; j++) *(LAS u4*)(uintptr_t)(ys + e * 2048 + (4 * j + (l >> 4)) * 256 + (((l & 15) ^ (((l >> 4) & 1) << 3)) << 4)) = y[e][j];
#pragma unroll
  for (int e = 0; e < 4; e++) {
    if (e < ns) {
      bf8 yb[2], ga[2];
#pragma unroll
      for (int T = 0; T < 2; T++) {
        const unsigned a = ys + e * 2048 + (4 * h + (q >> 1)) * 256 + (((8 * T + 4 * dh + pp) ^ ((q >> 1) << 3)) << 4) + (q & 1) * 8;
        yb[T] = cat(ldstr(a), ldstr(a + 512));
        const int d = 64 * ct + 32 * T + (l & 31), k0 = 8 * (st0 + e) + 4 * h;
        const float a0 = (float)((k0 * d) % 448) * (1.0f / 448.0f), dd = (float)d * (1.0f / 448.0f);
        float v[8];
#pragma unroll
        for (int j = 0; j < 8; j++) v[j] = cosr(a0 + (float)(j >> 1) * dd + ((j & 1) ? 0.25f : 0.f));
        if (k0 == 0) v[1] = (d & 1) ? -1.f : 1.f;
        ga[T] = asbf8(u4{pk(v[0], v[1]), pk(v[2], v[3]), pk(v[4], v[5]), pk(v[6], v[7])});
      }
#pragma unroll
      for (int Td = 0; Td < 2; Td++)
#pragma unroll
        for (int Ts = 0; Ts < 2; Ts++) acc[Td][Ts] = mfma32(ga[Td], yb[Ts], acc[Td][Ts]);
    }
  }
  const int row = 8 * w + (l >> 3), dg = l & 7;
  const size_t go = (size_t)(32 * row + s2) * 896 + ct * 128 + dg * 16;
  const u4 xr = *(const u4*)(p.x + go);
  const u4 bb = *(const u4*)(p.b2 + ct * 128 + dg * 16);
  WAITL0();
  BAR();
  STAMP(0, 14);
#pragma unroll
  for (int Td = 0; Td < 2; Td++)
#pragma unroll
    for (int Ts = 0; Ts < 2; Ts++)
#pragma unroll
      for (int g = 0; g < 4; g++) {
        const f16v& a = acc[Td][Ts];
        *(LAS f4*)(uintptr_t)(w * 16384 + (32 * Ts + (l & 31)) * 256 + (((8 * Td + 2 * g + h) ^ (l & 7)) << 4)) = f4{a[4 * g], a[4 * g + 1], a[4 * g + 2], a[4 * g + 3]};
      }
  WAITL0();
  BAR();
  f4 s0 = {bflo(xr[0]) + bflo(bb[0]), bfhi(xr[0]) + bfhi(bb[0]), bflo(xr[1]) + bflo(bb[1]), bfhi(xr[1]) + bfhi(bb[1])};
  f4 s1 = {bflo(xr[2]) + bflo(bb[2]), bfhi(xr[2]) + bfhi(bb[2]), bflo(xr[3]) + bflo(bb[3]), bfhi(xr[3]) + bfhi(bb[3])};
#pragma unroll
  for (int v = 0; v < 8; v++) {
    s0 += *(const LAS f4*)(uintptr_t)(v * 16384 + row * 256 + (((2 * dg) ^ (row & 7)) << 4));
    s1 += *(const LAS f4*)(uintptr_t)(v * 16384 + row * 256 + (((2 * dg + 1) ^ (row & 7)) << 4));
  }
  *(u4*)(p.out + go) = asu4(pack44(s0, s1));
  WAITV(0);
  STAMP(0, 15);
}

DI u64 rt_() { u64 t; asm volatile("s_memrealtime %0\n\ts_waitcnt lgkmcnt(0)" : "=s"(t) :: "memory"); return t; }
DI u64 ct_() { u64 t; asm volatile("s_memtime %0\n\ts_waitcnt lgkmcnt(0)" : "=s"(t) :: "memory"); return t; }
DI void glds16b(unsigned voff, const char* sbase, unsigned lds) {
  asm volatile("s_mov_b32 m0, %2\n\ts_nop 0\n\tglobal_load_lds_dwordx4 %0, %1" :: "v"(voff), "s"(sbase), "s"(lds) : "memory");
}
constexpr int WIN = 12;
__global__ __launch_bounds__(512) void k_probe(Params p) {
  u64 r0 = rt_(); u64 c0 = ct_();
  const char* x = p.x; const char* w1 = p.w1; const char* w2 = p.w2;
  u64* st = (u64*)(p.ws + (64 << 20));
  int b = blockIdx.x, xc = b & 7, i = b >> 3, l = threadIdx.x & 63, w = __builtin_amdgcn_readfirstlane(threadIdx.x >> 6);
  int j = (xc & 1) * 4 + (i & 3), g = (xc >> 1) * 8 + (i >> 2);
  u64 t[3] = {0, 0, 0};
  if (w < 4) {
    unsigned slot = 0;
    int o1 = (j & 1) ? 64 : 0, os = (j & 1) ? 0 : 384;
    const unsigned ldsb = w * 32768;
#define ISS(off, base) do { glds16b(off, base, ldsb + slot); slot = (slot + 1024) & 32767; asm volatile("s_waitcnt vmcnt(%0)" :: "i"(WIN - 1) : "memory"); } while (0)
    { unsigned off0 = (32 * (16 * w + (l >> 3)) + g) * 896 + (l & 7) * 16, off1 = off0 + 256 * 896;
#pragma unroll
      for (int c = 0; c < 7; c++) { ISS(off0, x + c * 128); ISS(off1, x + c * 128); } }
    { unsigned off = (8 * w + (l >> 3)) * 3584 + 448 * j + o1 + (l & 7) * 16;
#pragma unroll 1
      for (int r = 0; r < 3; r++) {
#pragma unroll
        for (int m = 0; m < 14; m++) { ISS(off, w1 + r * 128 + m * 32 * 3584); if (r * 14 + m + 1 == WIN - 1) t[0] = ct_() - c0; }
      }
      off = (16 * w + (l >> 2)) * 3584 + 448 * j + os + (l & 3) * 16;
#pragma unroll
      for (int m = 0; m < 7; m++) ISS(off, w1 + m * 64 * 3584);
    }
    unsigned off = (224 * j + 8 * w + (l >> 3)) * 896 + (l & 7) * 16;
#pragma unroll 1
    for (int r = 0; r < 7; r++) {
#pragma unroll
      for (int m = 0; m < 7; m++) { ISS(off, w2 + r * 128 + m * 32 * 896); if (r * 7 + m + 1 == WIN - 1) t[1] = ct_() - c0; }
    }
    asm volatile("s_waitcnt vmcnt(0)" ::: "memory");
    t[2] = ct_() - c0;
  }
  __syncthreads();
  u64 r1 = rt_();
  if (threadIdx.x == 0) { u64* s = st + b * 8; s[0] = r0; s[1] = r1; s[2] = t[0]; s[3] = t[1]; s[4] = t[2]; }
}
__global__ void k_spin(Params p) {
  const u64* st = (const u64*)(p.ws + (64 << 20));
  u64 mn = ~0ull, mxs = 0, mxe = 0, sum = 0;
  for (int b = 0; b < 256; b++) { u64 a = st[b * 8]; mn = a < mn ? a : mn; mxs = a > mxs ? a : mxs; u64 e = st[b * 8 + 1]; mxe = e > mxe ? e : mxe; if (METRIC >= 1 && METRIC <= 3) sum += st[b * 8 + 1 + METRIC]; }
  u64 extra = METRIC == 0 ? 0 : METRIC <= 3 ? (sum / 256) / 5 : METRIC == 4 ? (mxe - mn) * 5 : (mxs - mn) * 5;
  if (extra > 20000) extra = 20000;
  u64 target = mn + 10000 + extra;
  for (int it = 0; it < 1000000; it++) if (rt_() >= target) break;
}
extern "C" void kernel_launch(void* const* d_in, const int* in_sizes, int n_in, void* d_out, int out_size, void* d_ws, size_t ws_size, hipStream_t stream) {
  Params p;
  p.x = (const char*)d_in[0]; p.wr = (const char*)d_in[1]; p.wi = (const char*)d_in[2]; p.w1 = (const char*)d_in[4];
  p.b1 = (const char*)d_in[5]; p.w2 = (const char*)d_in[6]; p.b2 = (const char*)d_in[7]; p.out = (char*)d_out; p.ws = (char*)d_ws;
  (void)hipFuncSetAttribute((const void*)k_s1, hipFuncAttributeMaxDynamicSharedMemorySize, L_TOTAL);
  (void)hipFuncSetAttribute((const void*)k_s3, hipFuncAttributeMaxDynamicSharedMemorySize, L_TOTAL);
  (void)hipFuncSetAttribute((const void*)k_probe, hipFuncAttributeMaxDynamicSharedMemorySize, L_TOTAL);
  k_probe<<<256, 512, L_TOTAL, stream>>>(p);
  k_s1<<<256, 512, L_TOTAL, stream>>>(p);
  k_s2<<<225, 256, 8192, stream>>>(p);
  k_s3<<<224, 512, L_TOTAL, stream>>>(p);
  k_spin<<<1, 64, 0, stream>>>(p);
}
```

```cpp
#define METRIC 3
#define PF 1
#include <hip/hip_runtime.h>
#include <stdint.h>

typedef __bf16 bf2 __attribute__((ext_vector_type(2)));
typedef __bf16 bf4 __attribute__((ext_vector_type(4)));
typedef __bf16 bf8 __attribute__((ext_vector_type(8)));
typedef float f2 __attribute__((ext_vector_type(2)));
typedef float f4 __attribute__((ext_vector_type(4)));
typedef float f16v __attribute__((ext_vector_type(16)));
typedef unsigned u2 __attribute__((ext_vector_type(2)));
typedef unsigned u4 __attribute__((ext_vector_type(4)));
typedef unsigned long long u64;
#define LAS __attribute__((address_space(3)))
#define DI __device__ __forceinline__
typedef __amdgpu_buffer_rsrc_t rsrc_t;

constexpr int WS_WT = 65536;
constexpr int WS_X1 = WS_WT + 232 * 8192;
constexpr int WS_Y1 = WS_X1 + 224 * 8192;
constexpr int WS_H = WS_Y1 + 224 * 8192;
constexpr int WS_END = WS_H + 32 * 112 * 2048;
constexpr int AUX = 0;

#define STAMP(w, i)

struct Params {
  const char* x; const char* wr; const char* wi; const char* w1; const char* b1; const char* w2; const char* b2;
  char* out; char* ws;
};

DI unsigned pk(float a, float b) { f2 v = {a, b}; bf2 r = __builtin_convertvector(v, bf2); return __builtin_bit_cast(unsigned, r); }
DI float rbf(float a) { return __uint_as_float(pk(a, a) << 16); }
DI float bflo(unsigned u) { return __uint_as_float(u << 16); }
DI float bfhi(unsigned u) { return __uint_as_float(u & 0xffff0000u); }
DI bf8 asbf8(u4 v) { return __builtin_bit_cast(bf8, v); }
DI u4 asu4(bf8 v) { return __builtin_bit_cast(u4, v); }
DI float dot2(unsigned a, unsigned b, float c) { return __builtin_amdgcn_fdot2_f32_bf16(__builtin_bit_cast(bf2, a), __builtin_bit_cast(bf2, b), c, false); }
DI f16v mfma32(bf8 a, bf8 b, f16v c) { return __builtin_amdgcn_mfma_f32_32x32x16_bf16(a, b, c, 0, 0, 0); }
DI f4 mfma16(bf8 a, bf8 b, f4 c) { return __builtin_amdgcn_mfma_f32_16x16x32_bf16(a, b, c, 0, 0, 0); }
DI bf8 lds128(unsigned a) { return *(const LAS bf8*)(uintptr_t)a; }
DI bf4 ldstr(unsigned a) { return __builtin_amdgcn_ds_read_tr16_b64_v4bf16((LAS bf4*)(uintptr_t)a); }
DI bf8 cat(bf4 a, bf4 b) { return __builtin_shufflevector(a, b, 0, 1, 2, 3, 4, 5, 6, 7); }
DI float xhalf(float v) { unsigned u = __float_as_uint(v); u2 r = __builtin_amdgcn_permlane32_swap(u, u, false, false); unsigned a = r[0], b = r[1]; return __uint_as_float(a) + __uint_as_float(b); }
DI float cosr(float rev) { return __builtin_amdgcn_cosf(rev); }
DI float sinr(float rev) { return __builtin_amdgcn_sinf(rev); }
DI void glds16(unsigned voff, const char* sbase, unsigned lds) {
  unsigned keep;
  asm volatile("s_mov_b32 %0, m0\n\ts_mov_b32 m0, %3\n\ts_nop 0\n\tglobal_load_lds_dwordx4 %1, %2\n\ts_mov_b32 m0, %0" : "=&s"(keep) : "v"(voff), "s"(sbase), "s"(lds) : "memory");
}
DI void glds4(unsigned voff, const char* sbase, unsigned lds) {
  unsigned keep;
  asm volatile("s_mov_b32 %0, m0\n\ts_mov_b32 m0, %3\n\ts_nop 0\n\tglobal_load_lds_dword %1, %2\n\ts_mov_b32 m0, %0" : "=&s"(keep) : "v"(voff), "s"(sbase), "s"(lds) : "memory");
}
#define WAITV(n) asm volatile("s_waitcnt vmcnt(" #n ")" ::: "memory")
#define WAITL0() asm volatile("s_waitcnt lgkmcnt(0)" ::: "memory")
#define BAR() do { asm volatile("" ::: "memory"); __builtin_amdgcn_s_barrier(); asm volatile("" ::: "memory"); } while (0)
#define SCHED() __builtin_amdgcn_sched_barrier(0)

constexpr unsigned L_DFT = 131072;
constexpr unsigned L_B1 = L_DFT + 28672;
constexpr unsigned L_TOTAL = 163840;

DI u4 gelu8(const f16v& a, int o, float r) {
  float g[8];
#pragma unroll
  for (int i = 0; i < 8; i++) {
    float u = a[o + i] * r;
    float z = u * __builtin_fmaf(u * u, -0.10294324f, -2.3022082f);
    g[i] = u * __builtin_amdgcn_rcpf(1.0f + __builtin_amdgcn_exp2f(z));
  }
  return u4{pk(g[0], g[1]), pk(g[2], g[3]), pk(g[4], g[5]), pk(g[6], g[7])};
}
DI bf8 split3(float c, unsigned bias) {
  float hi = rbf(c), mid = rbf(c - hi), lo = c - hi - mid;
  return asbf8(u4{pk(hi, mid), (pk(lo, lo) & 0xffffu) | (bias << 16), 0u, 0u});
}

template <int G> struct Frag { bf8 x; bf8 w[G == 0 ? 3 : 2]; };

template <int G>
DI void s1_body(const Params& p, int rt, int ft, int s2o, int cto, int w, int l) {
  const int sg = w & 3, h = l >> 5;
  unsigned xoff[2], woff[2];
#pragma unroll
  for (int j = 0; j < 2; j++) {
    int row = 16 * w + 8 * j + (l >> 3);
    xoff[j] = (32 * (row & 63) + 16 * (row >> 6) + rt) * 896 + (((l & 7) ^ ((row >> 1) & 7)) << 4);
    int kr = 8 * w + 4 * j + (l >> 4);
    woff[j] = kr * 3584 + ft * 256 + (((l & 15) ^ ((kr & 3) << 2)) << 4);
  }
  const unsigned w2off = (224 * w + (l >> 3)) * 896 + cto * 128 + (((l & 7) ^ (((l >> 4) & 1) << 2)) << 4);
  if (w == 0) glds4(l * 4, p.b1 + ft * 256, L_B1);
#pragma unroll
  for (int c = 0; c < 4; c++) {
#pragma unroll
    for (int j = 0; j < 2; j++) glds16(xoff[j], p.x + c * 128, c * 32768 + (2 * w + j) * 1024);
#pragma unroll
    for (int j = 0; j < 2; j++) glds16(woff[j], p.w1 + c * 64 * 3584, c * 32768 + 16384 + (2 * w + j) * 1024);
  }
  {
    int c = l & 31, k = 16 * ft + (c & 15);
    float ph = (c >> 4) ? 0.25f : 0.0f;
    if (k == 0 && c == 16) { k = 224; ph = 0.f; }
    const float dk = (float)k * (1.0f / 448.0f);
    for (int st = w; st < 28; st += 8) {
      int b0 = ((16 * st + 8 * h) * k) % 448;
      float a0 = __builtin_fmaf((float)b0, 1.0f / 448.0f, ph);
      float v[8];
#pragma unroll
      for (int j = 0; j < 8; j++) v[j] = cosr(__builtin_fmaf((float)j, dk, a0));
      *(LAS u4*)(uintptr_t)(L_DFT + st * 1024 + l * 16) = u4{pk(v[0], v[1]), pk(v[2], v[3]), pk(v[4], v[5]), pk(v[6], v[7])};
    }
  }
  unsigned xa[4], wa[G == 0 ? 3 : 1];
  {
    int row = 32 * sg + (l & 31);
#pragma unroll
    for (int v = 0; v < 4; v++) xa[v] = row * 128 + (((2 * v + h) ^ ((row >> 1) & 7)) << 4);
    int q = (l >> 2) & 3, pp = l & 3, fh = (l >> 4) & 1;
#pragma unroll
    for (int t = 0; t < (G == 0 ? 3 : 1); t++) {
      int T = G == 0 ? t : 3;
      wa[t] = 16384 + (8 * h + q) * 256 + (((4 * T + 2 * fh + (pp >> 1)) ^ (q << 2)) << 4) + (pp & 1) * 8;
    }
  }
  f16v acc[G == 0 ? 3 : 2];
#pragma unroll
  for (int t = 0; t < (G == 0 ? 3 : 2); t++)
#pragma unroll
    for (int i = 0; i < 16; i++) acc[t][i] = 0.f;
  float sx0 = 0.f, sx1 = 0.f, sq0 = 0.f, sq1 = 0.f, cs[G == 0 ? 3 : 2];
#pragma unroll
  for (int t = 0; t < (G == 0 ? 3 : 2); t++) cs[t] = 0.f;

  auto load = [&](Frag<G>& f, unsigned base, unsigned dbase, int v) {
    f.x = lds128(base + xa[v]);
#pragma unroll
    for (int t = 0; t < (G == 0 ? 3 : 1); t++) f.w[t] = cat(ldstr(base + wa[t] + v * 4096), ldstr(base + wa[t] + v * 4096 + 1024));
    if (G == 1) f.w[1] = lds128(dbase + v * 1024);
  };
  auto mma = [&](const Frag<G>& f, int v) {
    if (G == 0) {
#pragma unroll
      for (int t = 0; t < 3; t++) acc[t] = mfma32(f.w[t], f.x, acc[t]);
    } else {
      acc[0] = mfma32(f.w[0], f.x, acc[0]);
      acc[1] = mfma32(f.x, f.w[1], acc[1]);
    }
    u4 xu = asu4(f.x);
    sx0 = dot2(xu[0], 0x3f803f80u, sx0); sq0 = dot2(xu[0], xu[0], sq0);
    sx1 = dot2(xu[1], 0x3f803f80u, sx1); sq1 = dot2(xu[1], xu[1], sq1);
    sx0 = dot2(xu[2], 0x3f803f80u, sx0); sq0 = dot2(xu[2], xu[2], sq0);
    sx1 = dot2(xu[3], 0x3f803f80u, sx1); sq1 = dot2(xu[3], xu[3], sq1);
    if (v == sg) {
#pragma unroll
      for (int i = 0; i < 4; i++)
#pragma unroll
        for (int t = 0; t < (G == 0 ? 3 : 1); t++) cs[t] = dot2(asu4(f.w[t])[i], 0x3f803f80u, cs[t]);
    }
    if (G == 1) {
#pragma unroll
      for (int i = 0; i < 4; i++) cs[1] = dot2(asu4(f.w[1])[i], 0x3f803f80u, cs[1]);
    }
  };
  STAMP(0, 1);
  asm volatile("s_waitcnt vmcnt(12) lgkmcnt(0)" ::: "memory");
  BAR();
  STAMP(0, 2);
  Frag<G> fa, fb;
  load(fa, 0, L_DFT + l * 16, 0);
#pragma unroll 1
  for (int c = 0; c < 7; c++) {
    const unsigned base = (c & 3) * 32768, db = L_DFT + l * 16 + c * 4096;
    load(fb, base, db, 1); mma(fa, 0); SCHED();
    load(fa, base, db, 2); mma(fb, 1); SCHED();
    load(fb, base, db, 3); mma(fa, 2); SCHED();
    if (c < 6) asm volatile("s_waitcnt vmcnt(8) lgkmcnt(0)" ::: "memory"); else WAITL0();
    BAR();
    if (c < 3) {
#pragma unroll
      for (int j = 0; j < 2; j++) glds16(xoff[j], p.x + (c + 4) * 128, base + (2 * w + j) * 1024);
#pragma unroll
      for (int j = 0; j < 2; j++) glds16(woff[j], p.w1 + (c + 4) * 64 * 3584, base + 16384 + (2 * w + j) * 1024);
    } else {
#pragma unroll
      for (int j = 0; j < 4; j++) glds16(w2off, p.w2 + ((c - 3) * 32 + j * 8) * 896, base + w * 4096 + j * 1024);
    }
    if (c < 6) load(fa, ((c + 1) & 3) * 32768, db + 4096, 0);
    mma(fb, 3); SCHED();
  }
  STAMP(0, 3);
#pragma unroll
  for (int t = 0; t < (G == 0 ? 3 : 1); t++) *(LAS float*)(uintptr_t)(L_DFT + ((sg * 4 + (G == 0 ? t : 3)) * 64 + l) * 4) = cs[t];
  const float sum = xhalf(sx0 + sx1), ssq = xhalf(sq0 + sq1);
  const float mean = sum * (1.0f / 448.0f);
  const float var = __builtin_fmaxf(__builtin_fmaf(-mean, mean, ssq * (1.0f / 448.0f)), 0.f);
  const float sd = rbf(__builtin_sqrtf(rbf(rbf(var) + 1e-5f)));
  const float r = 1.0f / sd;
  const bf8 ext = asbf8(u4{pk(-mean, -mean), pk(-mean, sd), 0u, 0u});
  if (G == 1 && h == 0) *(LAS float*)(uintptr_t)(L_DFT + 4096 + sg * 128 + l * 4) = r;
  WAITL0();
  BAR();
  const rsrc_t rws = __builtin_amdgcn_make_buffer_rsrc(p.ws, 0, WS_END, 0x00020000);
  const int s2 = 16 * (sg >> 1) + rt;
  if (G == 1) {
    acc[1] = mfma32(ext, split3(cs[1], 0u), acc[1]);
    const int c = l & 31;
    const unsigned o = WS_X1 + (16 * ft + (c & 15)) * 8192 + s2 * 256 + (sg & 1) * 128 + (c >> 4) * 32 + h * 16;
#pragma unroll
    for (int t = 0; t < 2; t++) {
      f4 r0 = *(const LAS f4*)(uintptr_t)(L_DFT + 4096 + sg * 128 + (16 * t + 4 * h) * 4);
      f4 r1 = *(const LAS f4*)(uintptr_t)(L_DFT + 4096 + sg * 128 + (16 * t + 8 + 4 * h) * 4);
      const f16v& a = acc[1];
      u4 v = {pk(a[8 * t] * r0[0], a[8 * t + 1] * r0[1]), pk(a[8 * t + 2] * r0[2], a[8 * t + 3] * r0[3]),
              pk(a[8 * t + 4] * r1[0], a[8 * t + 5] * r1[1]), pk(a[8 * t + 6] * r1[2], a[8 * t + 7] * r1[3])};
      __builtin_amdgcn_raw_buffer_store_b128(v, rws, o + t * 64, 0, AUX);
    }
  }
#pragma unroll
  for (int t = 0; t < (G == 0 ? 3 : 1); t++) {
    const int T = G == 0 ? t : 3;
    float c = 0.f;
#pragma unroll
    for (int s = 0; s < 4; s++) c += *(const LAS float*)(uintptr_t)(L_DFT + ((s * 4 + T) * 64 + l) * 4);
    unsigned bias = *(const LAS unsigned short*)(uintptr_t)(L_B1 + (T * 32 + (l & 31)) * 2);
    acc[t] = mfma32(split3(c, h ? 0u : bias), ext, acc[t]);
  }
#pragma unroll
  for (int t = 0; t < (G == 0 ? 3 : 1); t++) {
    const int T = G == 0 ? t : 3;
    const unsigned o = WS_H + (((s2 * 112 + ft * 8 + T * 2) * 2 + (sg & 1)) * 64 + l) * 16;
    __builtin_amdgcn_raw_buffer_store_b128(gelu8(acc[t], 0, r), rws, o, 0, AUX);
    __builtin_amdgcn_raw_buffer_store_b128(gelu8(acc[t], 8, r), rws, o + 2048, 0, AUX);
  }
  STAMP(0, 4);
}

DI void helper_wt(const Params& p, int id) {
  const int tid = threadIdx.x;
  const int w4 = id >> 3, f20 = (id & 7) * 4;
  for (int e = tid; e < 3600; e += 512) {
    int seg = e / 450, o = e - seg * 450;
    const char* src = (seg >= 4 ? p.wi : p.wr) + (size_t)(16 * w4 + 64 * (f20 + (seg & 3))) * 450 + o * 16;
    *(LAS u4*)(uintptr_t)(e * 16) = *(const u4*)src;
  }
  __syncthreads();
  const rsrc_t rws = __builtin_amdgcn_make_buffer_rsrc(p.ws, 0, WS_END, 0x00020000);
  for (int e = tid; e < 3600; e += 512) {
    int k = e >> 4, c = e & 15;
    u4 v;
#pragma unroll
    for (int i = 0; i < 4; i++) {
      unsigned a = *(const LAS unsigned short*)(uintptr_t)(i * 7200 + c * 450 + k * 2);
      unsigned b = *(const LAS unsigned short*)(uintptr_t)(28800 + i * 7200 + c * 450 + k * 2);
      v[i] = a | (b << 16);
    }
    __builtin_amdgcn_raw_buffer_store_b128(v, rws, WS_WT + k * 8192 + id * 256 + c * 16, 0, AUX);
  }
}

__global__ __launch_bounds__(512, 2) void k_s1(Params p) {
  extern __shared__ __attribute__((aligned(16))) char smem[];
  STAMP(0, 0);
  const int b = blockIdx.x, xc = b & 7, i = b >> 3;
  const int l = threadIdx.x & 63, w = __builtin_amdgcn_readfirstlane(threadIdx.x >> 6);
  if (i >= 28) { helper_wt(p, xc * 4 + (i - 28)); return; }
  const int rt = (xc >> 1) * 4 + i / 7, ft = (xc & 1) * 7 + i % 7;
  const int s2o = xc * 4 + i / 7, cto = i % 7;
  if (w < 4) s1_body<0>(p, rt, ft, s2o, cto, w, l); else s1_body<1>(p, rt, ft, s2o, cto, w, l);
  WAITV(0);
  STAMP(0, 5);
}

DI bf8 gen8(float a0, float da_lo, float da_hi) {
  float v[8];
#pragma unroll
  for (int j = 0; j < 8; j++) v[j] = cosr(a0 + (float)(j & 3) * da_lo + (float)(j >> 2) * da_hi);
  return asbf8(u4{pk(v[0], v[1]), pk(v[2], v[3]), pk(v[4], v[5]), pk(v[6], v[7])});
}
DI bf8 neg8(bf8 a) { u4 v = asu4(a); return asbf8(v ^ 0x80008000u); }
DI bf8 pack44(f4 a, f4 b) { return asbf8(u4{pk(a[0], a[1]), pk(a[2], a[3]), pk(b[0], b[1]), pk(b[2], b[3])}); }

DI void fft_col(const Params& p, int k, int w4, int l, unsigned lds) {
  const rsrc_t rws = __builtin_amdgcn_make_buffer_rsrc(p.ws, 0, WS_END, 0x00020000);
  const int c = l & 15, q = l >> 4;
  const bool special = (k == 0) || (k == 224);
  const int kb = k == 224 ? 0 : k;
  bf8 z[2][2][2];
  {
    const unsigned o = WS_X1 + kb * 8192 + c * 256 + (q >> 1) * 64 + (q & 1) * 16 + (k == 224 ? 32 : 0);
#pragma unroll
    for (int u = 0; u < 2; u++)
#pragma unroll
      for (int hf = 0; hf < 2; hf++) {
        z[u][0][hf] = asbf8(__builtin_amdgcn_raw_buffer_load_b128(rws, o + u * 4096 + hf * 128, 0, AUX));
        u4 t = __builtin_amdgcn_raw_buffer_load_b128(rws, (o | 32) + u * 4096 + hf * 128, 0, AUX);
        z[u][1][hf] = asbf8(special ? u4{0u, 0u, 0u, 0u} : t);
      }
  }
  u4 wt[2];
#pragma unroll
  for (int u = 0; u < 2; u++) wt[u] = __builtin_amdgcn_raw_buffer_load_b128(rws, WS_WT + k * 8192 + (w4 * 2 + u) * 1024 + l * 16, 0, AUX);
  bf8 C64[2], S64[2], N64[2], C32[2], S32[2], N32[2];
  const float f1 = (float)(16 * w4 + c);
#pragma unroll
  for (int e = 0; e < 2; e++) {
    float d = f1 * (1.0f / 64.0f);
    float a0 = d * (float)(32 * e + 16 * (q >> 1) + 4 * (q & 1));
    C64[e] = gen8(a0, d, 8.f * d); S64[e] = gen8(a0 - 0.25f, d, 8.f * d); N64[e] = neg8(S64[e]);
    float d2 = (float)(16 * e + c) * (1.0f / 32.0f);
    float b0 = d2 * (float)(4 * q);
    C32[e] = gen8(b0, d2, 16.f * d2); S32[e] = gen8(b0 - 0.25f, d2, 16.f * d2); N32[e] = neg8(S32[e]);
  }
  float tc[2][4], ts[2][4], ic[2][4], is[2][4];
  const float scale = (special ? 1.0f : 2.0f) / (2048.0f * 448.0f);
#pragma unroll
  for (int u = 0; u < 2; u++)
#pragma unroll
    for (int i = 0; i < 4; i++) {
      float a = (float)(16 * u + 4 * q + i) * f1 * (1.0f / 2048.0f);
      tc[u][i] = cosr(a); ts[u][i] = sinr(a);
      float b = (float)((16 * u + c) * (16 * w4 + 4 * q + i)) * (1.0f / 2048.0f);
      ic[u][i] = cosr(b) * scale; is[u][i] = sinr(b) * scale;
    }
  const f4 zero = {0.f, 0.f, 0.f, 0.f};
  f4 re[2], im[2];
#pragma unroll
  for (int u = 0; u < 2; u++) {
    f4 a = mfma16(z[u][0][0], C64[0], zero), b = mfma16(z[u][1][0], C64[0], zero);
    a = mfma16(z[u][0][1], C64[1], a); b = mfma16(z[u][1][1], C64[1], b);
    a = mfma16(z[u][1][0], S64[0], a); b = mfma16(z[u][0][0], N64[0], b);
    a = mfma16(z[u][1][1], S64[1], a); b = mfma16(z[u][0][1], N64[1], b);
#pragma unroll
    for (int i = 0; i < 4; i++) { re[u][i] = a[i] * tc[u][i] + b[i] * ts[u][i]; im[u][i] = b[i] * tc[u][i] - a[i] * ts[u][i]; }
  }
  bf8 pr = pack44(re[0], re[1]), pi = pack44(im[0], im[1]);
#pragma unroll
  for (int u = 0; u < 2; u++) {
    f4 a = mfma16(C32[u], pr, zero), b = mfma16(C32[u], pi, zero);
    a = mfma16(S32[u], pi, a); b = mfma16(N32[u], pr, b);
#pragma unroll
    for (int i = 0; i < 4; i++) { float wr = bflo(wt[u][i]), wi = bfhi(wt[u][i]); re[u][i] = a[i] * wr - b[i] * wi; im[u][i] = a[i] * wi + b[i] * wr; }
  }
  pr = pack44(re[0], re[1]); pi = pack44(im[0], im[1]);
  {
    const unsigned wo = lds + (w4 >> 1) * 1024 + ((2 * (w4 & 1) + (q & 1)) * 16 + c) * 16 + (q >> 1) * 8;
#pragma unroll
    for (int u = 0; u < 2; u++) {
      f4 a = mfma16(pr, C32[u], zero), b = mfma16(pi, C32[u], zero);
      a = mfma16(pi, N32[u], a); b = mfma16(pr, S32[u], b);
      float xr[4], xi[4];
#pragma unroll
      for (int i = 0; i < 4; i++) { xr[i] = a[i] * ic[u][i] - b[i] * is[u][i]; xi[i] = b[i] * ic[u][i] + a[i] * is[u][i]; }
      *(LAS u2*)(uintptr_t)(wo + (u * 2 + 0) * 2048) = u2{pk(xr[0], xr[1]), pk(xr[2], xr[3])};
      *(LAS u2*)(uintptr_t)(wo + (u * 2 + 1) * 2048) = u2{pk(xi[0], xi[1]), pk(xi[2], xi[3])};
    }
  }
  __syncthreads();
#pragma unroll
  for (int u = 0; u < 2; u++) {
    bf8 ur0 = lds128(lds + (u * 2 + 0) * 2048 + l * 16), ur1 = lds128(lds + (u * 2 + 0) * 2048 + 1024 + l * 16);
    bf8 ui0 = lds128(lds + (u * 2 + 1) * 2048 + l * 16), ui1 = lds128(lds + (u * 2 + 1) * 2048 + 1024 + l * 16);
    f4 a = mfma16(C64[0], ur0, zero), b = mfma16(C64[0], ui0, zero);
    a = mfma16(C64[1], ur1, a); b = mfma16(C64[1], ui1, b);
    a = mfma16(N64[0], ui0, a); b = mfma16(S64[0], ur0, b);
    a = mfma16(N64[1], ui1, a); b = mfma16(S64[1], ur1, b);
    const unsigned o = WS_Y1 + kb * 8192 + (16 * u + c) * 256 + w4 * 64 + q * 16;
    u4 v = asu4(pack44(a, b));
    if (!special) __builtin_amdgcn_raw_buffer_store_b128(v, rws, o, 0, AUX);
    else __builtin_amdgcn_raw_buffer_store_b64(u2{v[0], v[1]}, rws, o + (k == 224 ? 8 : 0), 0, AUX);
  }
}

__global__ __launch_bounds__(256) void k_s2(Params p) {
  extern __shared__ __attribute__((aligned(16))) char smem[];
  STAMP(0, 8);
  fft_col(p, blockIdx.x, __builtin_amdgcn_readfirstlane(threadIdx.x >> 6), threadIdx.x & 63, 0);
  WAITV(0);
  STAMP(0, 9);
}

__global__ __launch_bounds__(512, 2) void k_s3(Params p) {
  extern __shared__ __attribute__((aligned(16))) char smem[];
  STAMP(0, 12);
  const int b = blockIdx.x, xc = b & 7, i = b >> 3;
  const int l = threadIdx.x & 63, w = __builtin_amdgcn_readfirstlane(threadIdx.x >> 6), h = l >> 5;
  const int s2 = xc * 4 + i / 7, ct = i % 7;
  const rsrc_t rws = __builtin_amdgcn_make_buffer_rsrc(p.ws, 0, WS_END, 0x00020000);
  const unsigned w2off = (224 * w + (l >> 3)) * 896 + ct * 128 + (((l & 7) ^ (((l >> 4) & 1) << 2)) << 4);
  const unsigned mine = w * 20480;
#pragma unroll
  for (int pc = 0; pc < 5; pc++)
#pragma unroll
    for (int j = 0; j < 4; j++) glds16(w2off, p.w2 + (pc * 32 + j * 8) * 896, mine + pc * 4096 + j * 1024);
  const int q = (l >> 2) & 3, pp = l & 3, dh = (l >> 4) & 1;
  unsigned wa[2];
#pragma unroll
  for (int T = 0; T < 2; T++) wa[T] = mine + (4 * h + q) * 128 + (((4 * T + 2 * dh + (pp >> 1)) ^ ((q >> 1) << 2)) << 4) + (pp & 1) * 8;
  f16v acc[2][2];
#pragma unroll
  for (int a = 0; a < 2; a++)
#pragma unroll
    for (int c = 0; c < 2; c++)
#pragma unroll
      for (int e = 0; e < 16; e++) acc[a][c][e] = 0.f;
  const unsigned ho = WS_H + (s2 * 112 + 14 * w) * 2048 + l * 16;
  bf8 hf[14][2];
#pragma unroll
  for (int st = 0; st < 14; st++)
#pragma unroll
    for (int T = 0; T < 2; T++) {
      u4 t; asm volatile("buffer_load_dwordx4 %0, %1, %2, 0 offen offset:%3" : "=v"(t) : "v"(ho + (st >> 1) * 4096), "s"(rws), "i"((st & 1) * 2048 + T * 1024) : "memory");
      hf[st][T] = asbf8(t);
    }
  bf8 wf[2][2][2];
  asm volatile("s_waitcnt vmcnt(40)" ::: "memory");
  bf8 w01[4][2];
#pragma unroll
  for (int st = 0; st < 4; st++)
#pragma unroll
    for (int T = 0; T < 2; T++) w01[st][T] = cat(ldstr(wa[T] + st * 2048), ldstr(wa[T] + st * 2048 + 1024));
  WAITL0();
#pragma unroll
  for (int pc = 5; pc < 7; pc++)
#pragma unroll
    for (int j = 0; j < 4; j++) glds16(w2off, p.w2 + (pc * 32 + j * 8) * 896, mine + (pc - 5) * 4096 + j * 1024);
  const int ns = w < 4 ? 4 : 3, st0 = w < 4 ? 4 * w : 16 + 3 * (w - 4);
  u4 y[4][2];
#pragma unroll
  for (int e = 0; e < 4; e++)
#pragma unroll
    for (int j = 0; j < 2; j++) {
      int col = 8 * (st0 + (e < ns ? e : 0)) + 4 * j + (l >> 4);
      asm volatile("buffer_load_dwordx4 %0, %1, %2, 0 offen" : "=v"(y[e][j]) : "v"(WS_Y1 + col * 8192 + s2 * 256 + (l & 15) * 16), "s"(rws) : "memory");
    }
#pragma unroll
  for (int st = 0; st < 14; st++) {
    if (st < 10) asm volatile("s_waitcnt vmcnt(%2)" : "+v"(hf[st][0]), "+v"(hf[st][1]) : "i"(16 + 26 - 2 * st) : "memory");
    else asm volatile("s_waitcnt vmcnt(%2)" : "+v"(hf[st][0]), "+v"(hf[st][1]) : "i"(st < 12 ? 12 : 8) : "memory");
    bf8 a[2];
#pragma unroll
    for (int T = 0; T < 2; T++) {
      if (st < 4) a[T] = w01[st][T];
      else { const int o = (st < 10 ? st : st - 10) * 2048; a[T] = cat(ldstr(wa[T] + o), ldstr(wa[T] + o + 1024)); }
    }
#pragma unroll
    for (int Td = 0; Td < 2; Td++)
#pragma unroll
      for (int Ts = 0; Ts < 2; Ts++) acc[Td][Ts] = mfma32(a[Td], hf[st][Ts], acc[Td][Ts]);
  }
  STAMP(0, 13);
  asm volatile("s_waitcnt vmcnt(0)" : "+v"(y[0][0]), "+v"(y[0][1]), "+v"(y[1][0]), "+v"(y[1][1]), "+v"(y[2][0]), "+v"(y[2][1]), "+v"(y[3][0]), "+v"(y[3][1]) :: "memory");
  const unsigned ys = mine + 8192;
#pragma unroll
  for (int e = 0; e < 4; e++)
#pragma unroll
    for (int j = 0; j < 2; j++) *(LAS u4*)(uintptr_t)(ys + e * 2048 + (4 * j + (l >> 4)) * 256 + (((l & 15) ^ (((l >> 4) & 1) << 3)) << 4)) = y[e][j];
#pragma unroll
  for (int e = 0; e < 4; e++) {
    if (e < ns) {
      bf8 yb[2], ga[2];
#pragma unroll
      for (int T = 0; T < 2; T++) {
        const unsigned a = ys + e * 2048 + (4 * h + (q >> 1)) * 256 + (((8 * T + 4 * dh + pp) ^ ((q >> 1) << 3)) << 4) + (q & 1) * 8;
        yb[T] = cat(ldstr(a), ldstr(a + 512));
        const int d = 64 * ct + 32 * T + (l & 31), k0 = 8 * (st0 + e) + 4 * h;
        const float a0 = (float)((k0 * d) % 448) * (1.0f / 448.0f), dd = (float)d * (1.0f / 448.0f);
        float v[8];
#pragma unroll
        for (int j = 0; j < 8; j++) v[j] = cosr(a0 + (float)(j >> 1) * dd + ((j & 1) ? 0.25f : 0.f));
        if (k0 == 0) v[1] = (d & 1) ? -1.f : 1.f;
        ga[T] = asbf8(u4{pk(v[0], v[1]), pk(v[2], v[3]), pk(v[4], v[5]), pk(v[6], v[7])});
      }
#pragma unroll
      for (int Td = 0; Td < 2; Td++)
#pragma unroll
        for (int Ts = 0; Ts < 2; Ts++) acc[Td][Ts] = mfma32(ga[Td], yb[Ts], acc[Td][Ts]);
    }
  }
  const int row = 8 * w + (l >> 3), dg = l & 7;
  const size_t go = (size_t)(32 * row + s2) * 896 + ct * 128 + dg * 16;
  const u4 xr = *(const u4*)(p.x + go);
  const u4 bb = *(const u4*)(p.b2 + ct * 128 + dg * 16);
  WAITL0();
  BAR();
  STAMP(0, 14);
#pragma unroll
  for (int Td = 0; Td < 2; Td++)
#pragma unroll
    for (int Ts = 0; Ts < 2; Ts++)
#pragma unroll
      for (int g = 0; g < 4; g++) {
        const f16v& a = acc[Td][Ts];
        *(LAS f4*)(uintptr_t)(w * 16384 + (32 * Ts + (l & 31)) * 256 + (((8 * Td + 2 * g + h) ^ (l & 7)) << 4)) = f4{a[4 * g], a[4 * g + 1], a[4 * g + 2], a[4 * g + 3]};
      }
  WAITL0();
  BAR();
  f4 s0 = {bflo(xr[0]) + bflo(bb[0]), bfhi(xr[0]) + bfhi(bb[0]), bflo(xr[1]) + bflo(bb[1]), bfhi(xr[1]) + bfhi(bb[1])};
  f4 s1 = {bflo(xr[2]) + bflo(bb[2]), bfhi(xr[2]) + bfhi(bb[2]), bflo(xr[3]) + bflo(bb[3]), bfhi(xr[3]) + bfhi(bb[3])};
#pragma unroll
  for (int v = 0; v < 8; v++) {
    s0 += *(const LAS f4*)(uintptr_t)(v * 16384 + row * 256 + (((2 * dg) ^ (row & 7)) << 4));
    s1 += *(const LAS f4*)(uintptr_t)(v * 16384 + row * 256 + (((2 * dg + 1) ^ (row & 7)) << 4));
  }
  *(u4*)(p.out + go) = asu4(pack44(s0, s1));
  WAITV(0);
  STAMP(0, 15);
}

DI u64 rt_() { u64 t; asm volatile("s_memrealtime %0\n\ts_waitcnt lgkmcnt(0)" : "=s"(t) :: "memory"); return t; }
DI u64 ct_() { u64 t; asm volatile("s_memtime %0\n\ts_waitcnt lgkmcnt(0)" : "=s"(t) :: "memory"); return t; }
DI void glds16b(unsigned voff, const char* sbase, unsigned lds) {
  asm volatile("s_mov_b32 m0, %2\n\ts_nop 0\n\tglobal_load_lds_dwordx4 %0, %1" :: "v"(voff), "s"(sbase), "s"(lds) : "memory");
}
constexpr int WIN = 12;

#define R8(x) x x x x x x x x
#define R64(x) R8(R8(x))
#define R512(x) R8(R64(x))
__global__ __launch_bounds__(512) void k_probe(Params p) {
  u64 r0 = rt_();
  u64* st = (u64*)(p.ws + (64 << 20));
  float a = threadIdx.x, b = 1.5f;
  if (PF) {
    u64 pc; asm volatile("s_getpc_b64 %0" : "=s"(pc));
    unsigned off = (threadIdx.x & 127) * 128, v;
    asm volatile("global_load_dword %0, %1, %2" : "=v"(v) : "v"(off), "s"(pc & ~127ull) : "memory");
    if (threadIdx.x >= 64) asm volatile("s_waitcnt vmcnt(0)" : "+v"(v) :: "memory");
    asm volatile("" :: "v"(v));
  }
  if (threadIdx.x >= 64) return;
  u64 t0 = ct_();
  asm volatile(R512("v_fma_f32 %0, %0, %1, %1\n\t") : "+v"(a) : "v"(b));
  u64 t1 = ct_();
  asm volatile(R512("v_fma_f32 %0, %0, %1, %1\n\t") : "+v"(a) : "v"(b));
  u64 t2 = ct_();
  asm volatile("s_branch 1f\n\t" R512("v_fma_f32 %0, %0, %1, %1\n\t") "1:\n\t" : "+v"(a) : "v"(b));
  u64 t3 = ct_();
  asm volatile(R512("v_fma_f32 %0, %0, %1, %1\n\t") : "+v"(a) : "v"(b));
  u64 t4 = ct_();
  asm volatile("s_waitcnt vmcnt(0)" ::: "memory");
  if (a == 0.12345f) st[9001] = 1;
  u64 r1 = rt_();
  if (threadIdx.x == 0) { u64* s = st + blockIdx.x * 8; s[0] = r0; s[1] = r1; s[2] = t1 - t0; s[3] = t2 - t1; s[4] = t3 - t2; }
}
__global__ void k_spin(Params p) {
  const u64* st = (const u64*)(p.ws + (64 << 20));
  u64 mn = ~0ull, mxs = 0, mxe = 0, sum = 0;
  for (int b = 0; b < 256; b++) { u64 a = st[b * 8]; mn = a < mn ? a : mn; mxs = a > mxs ? a : mxs; u64 e = st[b * 8 + 1]; mxe = e > mxe ? e : mxe; if (METRIC >= 1 && METRIC <= 3) sum += st[b * 8 + 1 + METRIC]; }
  u64 extra = METRIC == 0 ? 0 : METRIC <= 3 ? (sum / 256) / 5 : METRIC == 4 ? (mxe - mn) * 5 : (mxs - mn) * 5;
  if (extra > 20000) extra = 20000;
  u64 target = mn + 10000 + extra;
  for (int it = 0; it < 1000000; it++) if (rt_() >= target) break;
}
extern "C" void kernel_launch(void* const* d_in, const int* in_sizes, int n_in, void* d_out, int out_size, void* d_ws, size_t ws_size, hipStream_t stream) {
  Params p;
  p.x = (const char*)d_in[0]; p.wr = (const char*)d_in[1]; p.wi = (const char*)d_in[2]; p.w1 = (const char*)d_in[4];
  p.b1 = (const char*)d_in[5]; p.w2 = (const char*)d_in[6]; p.b2 = (const char*)d_in[7]; p.out = (char*)d_out; p.ws = (char*)d_ws;
  (void)hipFuncSetAttribute((const void*)k_s1, hipFuncAttributeMaxDynamicSharedMemorySize, L_TOTAL);
  (void)hipFuncSetAttribute((const void*)k_s3, hipFuncAttributeMaxDynamicSharedMemorySize, L_TOTAL);
  (void)hipFuncSetAttribute((const void*)k_probe, hipFuncAttributeMaxDynamicSharedMemorySize, L_TOTAL);
  k_probe<<<256, 128, L_TOTAL, stream>>>(p);
  k_s1<<<256, 512, L_TOTAL, stream>>>(p);
  k_s2<<<225, 256, 8192, stream>>>(p);
  k_s3<<<224, 512, L_TOTAL, stream>>>(p);
  k_spin<<<1, 64, 0, stream>>>(p);
}
```

```cpp
#include <hip/hip_runtime.h>
#include <stdint.h>

typedef __bf16 bf2 __attribute__((ext_vector_type(2)));
typedef __bf16 bf4 __attribute__((ext_vector_type(4)));
typedef __bf16 bf8 __attribute__((ext_vector_type(8)));
typedef float f2 __attribute__((ext_vector_type(2)));
typedef float f4 __attribute__((ext_vector_type(4)));
typedef float f16v __attribute__((ext_vector_type(16)));
typedef unsigned u2 __attribute__((ext_vector_type(2)));
typedef unsigned u4 __attribute__((ext_vector_type(4)));
typedef unsigned long long u64;
#define LAS __attribute__((address_space(3)))
#define DI __device__ __forceinline__
typedef __amdgpu_buffer_rsrc_t rsrc_t;

constexpr int WS_XF = 4096;
constexpr int WS_WF = WS_XF + 2048;
constexpr int WS_YF = 8192;
constexpr int WS_PF = 16384;
constexpr int WS_WT = 131072;
constexpr int WS_X1 = WS_WT + 232 * 8192;
constexpr int WS_Y1 = WS_X1 + 224 * 8192;
constexpr int WS_P = WS_Y1 + 224 * 8192;
constexpr int WS_END = WS_P + 32 * 7 * 6 * 8192;
constexpr int SC1 = 16;
constexpr unsigned L_FFT = 0, L_ACC = 8192, L_H = 24576, L_Y = 8192, L_RING = 57344;
constexpr unsigned L_B1 = 155648, L_MT = L_B1 + 1024, L_RT = L_MT + 256, L_CNT = L_RT + 256, L_ST = L_CNT + 256;
constexpr unsigned L_TOTAL = 163840;
constexpr int SPIN_CAP = 20000;
#define POLL_ROUND 6
#define CODE_LINES 280

struct Params {
  const char* x; const char* wr; const char* wi; const char* w1; const char* b1; const char* w2; const char* b2;
  char* out; char* ws;
};

#define STAMP(w, i)
#define STAMP_INIT()
#define STAMP_DUMP(w, lo)

DI unsigned pk(float a, float b) { f2 v = {a, b}; bf2 r = __builtin_convertvector(v, bf2); return __builtin_bit_cast(unsigned, r); }
DI unsigned pk2(f2 v) { bf2 r = __builtin_convertvector(v, bf2); return __builtin_bit_cast(unsigned, r); }
DI float rbf(float a) { return __uint_as_float(pk(a, a) << 16); }
DI float bflo(unsigned u) { return __uint_as_float(u << 16); }
DI float bfhi(unsigned u) { return __uint_as_float(u & 0xffff0000u); }
DI bf8 asbf8(u4 v) { return __builtin_bit_cast(bf8, v); }
DI u4 asu4(bf8 v) { return __builtin_bit_cast(u4, v); }
DI f16v mfma32(bf8 a, bf8 b, f16v c) { return __builtin_amdgcn_mfma_f32_32x32x16_bf16(a, b, c, 0, 0, 0); }
DI f4 mfma16(bf8 a, bf8 b, f4 c) { return __builtin_amdgcn_mfma_f32_16x16x32_bf16(a, b, c, 0, 0, 0); }
DI bf8 lds128(unsigned a) { return *(const LAS bf8*)(uintptr_t)a; }
DI f4 ldsf4(unsigned a) { return *(const LAS f4*)(uintptr_t)a; }
DI bf4 ldstr(unsigned a) { return __builtin_amdgcn_ds_read_tr16_b64_v4bf16((LAS bf4*)(uintptr_t)a); }
DI bf8 cat(bf4 a, bf4 b) { return __builtin_shufflevector(a, b, 0, 1, 2, 3, 4, 5, 6, 7); }
DI float cosr(float rev) { return __builtin_amdgcn_cosf(rev); }
DI float sinr(float rev) { return __builtin_amdgcn_sinf(rev); }
DI rsrc_t mkrs(const void* p, int n) { return __builtin_amdgcn_make_buffer_rsrc((void*)p, 0, n, 0x00020000); }
DI void dma16(rsrc_t rs, unsigned voff, unsigned soff, unsigned lds) {
  asm volatile("s_mov_b32 m0, %3\n\ts_nop 0\n\tbuffer_load_dwordx4 %0, %1, %2 offen lds" :: "v"(voff), "s"(rs), "s"(soff), "s"(lds) : "memory");
}
DI u4 ld16s(rsrc_t rs, unsigned voff, int imm) {
  u4 t; asm volatile("buffer_load_dwordx4 %0, %1, %2, 0 offen offset:%3 sc1" : "=v"(t) : "v"(voff), "s"(rs), "i"(imm) : "memory"); return t;
}
DI u2 ld8s(rsrc_t rs, unsigned voff) {
  u2 t; asm volatile("buffer_load_dwordx2 %0, %1, %2, 0 offen sc1" : "=v"(t) : "v"(voff), "s"(rs) : "memory"); return t;
}
DI void st16s(u4 v, rsrc_t rs, unsigned voff, int imm) { asm volatile("buffer_store_dwordx4 %0, %1, %2, 0 offen offset:%3 sc1\n\ts_nop 1" :: "v"(v), "v"(voff), "s"(rs), "i"(imm) : "memory"); }
DI void st8s(u2 v, rsrc_t rs, unsigned voff) { asm volatile("buffer_store_dwordx2 %0, %1, %2, 0 offen sc1" :: "v"(v), "v"(voff), "s"(rs) : "memory"); }
#define WAITV(n) asm volatile("s_waitcnt vmcnt(%0)" :: "i"(n) : "memory")
#define WAITVL(n) asm volatile("s_waitcnt vmcnt(%0) lgkmcnt(0)" :: "i"(n) : "memory")
#define WAITL0() asm volatile("s_waitcnt lgkmcnt(0)" ::: "memory")
#define TIE(x) asm volatile("" : "+v"(x))
#define BAR() do { asm volatile("" ::: "memory"); __builtin_amdgcn_s_barrier(); asm volatile("" ::: "memory"); } while (0)
#define SCHED() __builtin_amdgcn_sched_barrier(0)

DI bool match(u2 v, u2 tag) { return __builtin_amdgcn_ballot_w64(v[0] != tag[0] || v[1] != tag[1]) == 0; }
DI void poll(rsrc_t rs, unsigned voff, u2 tag) {
#pragma unroll 1
  for (int n = 0; n < SPIN_CAP; n++) {
    u2 v = ld8s(rs, voff);
    asm volatile("s_waitcnt vmcnt(0)" : "+v"(v) :: "memory");
    if (match(v, tag)) break;
    __builtin_amdgcn_s_sleep(1);
  }
}
DI u64 epoch_load(const char* ws) { u64 e; asm volatile("s_load_dwordx2 %0, %1, 0x0" : "=s"(e) : "s"(ws) : "memory"); return e; }
DI u2 epoch_tag(u64 e) {
  asm volatile("s_waitcnt lgkmcnt(0)" : "+s"(e) :: "memory");
  e = e * 6364136223846793005ull + 1442695040888963407ull;
  return u2{(unsigned)e, (unsigned)(e >> 32)};
}
DI void flag(rsrc_t rs, unsigned off, u2 tag, int l) { if (l == 0) st8s(tag, rs, off); }

DI u4 gelu8(f4 a, f4 b) {
  float g[8];
#pragma unroll
  for (int i = 0; i < 8; i++) {
    float u = i < 4 ? a[i & 3] : b[i & 3];
    float z = u * __builtin_fmaf(u * u, -0.10294324f, -2.3022082f);
    g[i] = u * __builtin_amdgcn_rcpf(1.0f + __builtin_amdgcn_exp2f(z));
  }
  return u4{pk(g[0], g[1]), pk(g[2], g[3]), pk(g[4], g[5]), pk(g[6], g[7])};
}
template <bool PAIRS> DI void gen14(bf8* out, float a0, float th, float dn) {
  f2 u[4], v[4];
#pragma unroll
  for (int i = 0; i < 8; i++) {
    float e = PAIRS ? __builtin_fmaf((float)(i >> 1), th, a0 + ((i & 1) ? 0.25f : 0.f)) : __builtin_fmaf((float)i, th, a0);
    u[i >> 1][i & 1] = cosr(e); v[i >> 1][i & 1] = cosr(e + dn);
  }
  const float c = 2.f * cosr(dn);
  const f2 c2 = {c, c};
#pragma unroll
  for (int n = 0; n < 14; n += 2) {
    out[n] = asbf8(u4{pk2(u[0]), pk2(u[1]), pk2(u[2]), pk2(u[3])});
    out[n + 1] = asbf8(u4{pk2(v[0]), pk2(v[1]), pk2(v[2]), pk2(v[3])});
    if (n < 12) {
#pragma unroll
      for (int i = 0; i < 4; i++) u[i] = __builtin_elementwise_fma(c2, v[i], -u[i]);
#pragma unroll
      for (int i = 0; i < 4; i++) v[i] = __builtin_elementwise_fma(c2, u[i], -v[i]);
    }
  }
}
DI u4 pack8(const f16v& a, int o) { return u4{pk(a[o], a[o + 1]), pk(a[o + 2], a[o + 3]), pk(a[o + 4], a[o + 5]), pk(a[o + 6], a[o + 7])}; }

DI bf8 gen8(float a0, float da_lo, float da_hi) {
  float v[8];
#pragma unroll
  for (int j = 0; j < 8; j++) v[j] = cosr(a0 + (float)(j & 3) * da_lo + (float)(j >> 2) * da_hi);
  return asbf8(u4{pk(v[0], v[1]), pk(v[2], v[3]), pk(v[4], v[5]), pk(v[6], v[7])});
}
DI bf8 neg8(bf8 a) { u4 v = asu4(a); return asbf8(v ^ 0x80008000u); }
DI bf8 pack44(f4 a, f4 b) { return asbf8(u4{pk(a[0], a[1]), pk(a[2], a[3]), pk(b[0], b[1]), pk(b[2], b[3])}); }
struct Fft {
  bf8 C64[2], S64[2], C32[2], S32[2];
  float tc[2][4], ts[2][4], ic[2][4], is[2][4];
  u4 z[2][2][2], wt[2];
  bf8 pr, pi;
  DI void consts(int w4, int l, bool special) {
    const int c = l & 15, q = l >> 4;
    const float f1 = (float)(16 * w4 + c);
#pragma unroll
    for (int e = 0; e < 2; e++) {
      float d = f1 * (1.0f / 64.0f);
      float a0 = d * (float)(32 * e + 16 * (q >> 1) + 4 * (q & 1));
      C64[e] = gen8(a0, d, 8.f * d); S64[e] = gen8(a0 - 0.25f, d, 8.f * d);
      float d2 = (float)(16 * e + c) * (1.0f / 32.0f);
      float b0 = d2 * (float)(4 * q);
      C32[e] = gen8(b0, d2, 16.f * d2); S32[e] = gen8(b0 - 0.25f, d2, 16.f * d2);
    }
    const float scale = (special ? 1.0f : 2.0f) / (2048.0f * 448.0f);
#pragma unroll
    for (int u = 0; u < 2; u++)
#pragma unroll
      for (int i = 0; i < 4; i++) {
        float a = (float)(16 * u + 4 * q + i) * f1 * (1.0f / 2048.0f);
        tc[u][i] = cosr(a); ts[u][i] = sinr(a);
        float b = (float)((16 * u + c) * (16 * w4 + 4 * q + i)) * (1.0f / 2048.0f);
        ic[u][i] = cosr(b) * scale; is[u][i] = sinr(b) * scale;
      }
  }
  DI void pin() {
#pragma unroll
    for (int e = 0; e < 2; e++) {
      TIE(C64[e]); TIE(S64[e]); TIE(C32[e]); TIE(S32[e]);
#pragma unroll
      for (int i = 0; i < 4; i++) { TIE(tc[e][i]); TIE(ts[e][i]); TIE(ic[e][i]); TIE(is[e][i]); }
    }
  }
  DI void loads(rsrc_t rws, int k, int w4, int l) {
    const int c = l & 15, q = l >> 4;
    const unsigned o = WS_X1 + (k == 224 ? 32 : k * 8192) + c * 256 + (q >> 1) * 64 + (q & 1) * 16, o2 = o + 4096;
#pragma unroll
    for (int hf = 0; hf < 2; hf++) {
      z[0][0][hf] = ld16s(rws, o, hf * 128); z[0][1][hf] = ld16s(rws, o, hf * 128 + 32);
      z[1][0][hf] = ld16s(rws, o2, hf * 128); z[1][1][hf] = ld16s(rws, o2, hf * 128 + 32);
    }
    const unsigned ow = WS_WT + k * 8192 + w4 * 2048 + l * 16;
    wt[0] = ld16s(rws, ow, 0); wt[1] = ld16s(rws, ow, 1024);
  }
  DI void arrived() {
#pragma unroll
    for (int u = 0; u < 2; u++) { TIE(z[u][0][0]); TIE(z[u][0][1]); TIE(z[u][1][0]); TIE(z[u][1][1]); TIE(wt[u]); }
  }
  DI void stageA(bool special) {
    const f4 zero = {0.f, 0.f, 0.f, 0.f};
    const bf8 N0 = neg8(S64[0]), N1 = neg8(S64[1]);
    f4 re[2], im[2];
#pragma unroll
    for (int u = 0; u < 2; u++) {
      const unsigned msk = special ? 0u : ~0u;
      bf8 r0 = asbf8(z[u][0][0]), r1 = asbf8(z[u][0][1]), i0 = asbf8(z[u][1][0] & msk), i1 = asbf8(z[u][1][1] & msk);
      f4 a = mfma16(r0, C64[0], zero), b = mfma16(i0, C64[0], zero);
      a = mfma16(r1, C64[1], a); b = mfma16(i1, C64[1], b);
      a = mfma16(i0, S64[0], a); b = mfma16(r0, N0, b);
      a = mfma16(i1, S64[1], a); b = mfma16(r1, N1, b);
#pragma unroll
      for (int i = 0; i < 4; i++) { re[u][i] = a[i] * tc[u][i] + b[i] * ts[u][i]; im[u][i] = b[i] * tc[u][i] - a[i] * ts[u][i]; }
    }
    pr = pack44(re[0], re[1]); pi = pack44(im[0], im[1]);
  }
  DI void stageB() {
    const f4 zero = {0.f, 0.f, 0.f, 0.f};
    const bf8 nr = neg8(pr);
    f4 re[2], im[2];
#pragma unroll
    for (int u = 0; u < 2; u++) {
      f4 a = mfma16(C32[u], pr, zero), b = mfma16(C32[u], pi, zero);
      a = mfma16(S32[u], pi, a); b = mfma16(S32[u], nr, b);
#pragma unroll
      for (int i = 0; i < 4; i++) { float wr = bflo(wt[u][i]), wi = bfhi(wt[u][i]); re[u][i] = a[i] * wr - b[i] * wi; im[u][i] = a[i] * wi + b[i] * wr; }
    }
    pr = pack44(re[0], re[1]); pi = pack44(im[0], im[1]);
  }
  DI void stageC(int w4, int l) {
    const f4 zero = {0.f, 0.f, 0.f, 0.f};
    const int c = l & 15, q = l >> 4;
    const bf8 ni = neg8(pi);
    const unsigned wo = L_FFT + (w4 >> 1) * 1024 + ((2 * (w4 & 1) + (q & 1)) * 16 + c) * 16 + (q >> 1) * 8;
#pragma unroll
    for (int u = 0; u < 2; u++) {
      f4 a = mfma16(pr, C32[u], zero), b = mfma16(pi, C32[u], zero);
      a = mfma16(ni, S32[u], a); b = mfma16(pr, S32[u], b);
      float xr[4], xi[4];
#pragma unroll
      for (int i = 0; i < 4; i++) { xr[i] = a[i] * ic[u][i] - b[i] * is[u][i]; xi[i] = b[i] * ic[u][i] + a[i] * is[u][i]; }
      *(LAS u2*)(uintptr_t)(wo + (u * 2 + 0) * 2048) = u2{pk(xr[0], xr[1]), pk(xr[2], xr[3])};
      *(LAS u2*)(uintptr_t)(wo + (u * 2 + 1) * 2048) = u2{pk(xi[0], xi[1]), pk(xi[2], xi[3])};
    }
  }
  DI void stageD(rsrc_t rws, int k, int w4, int l, bool special) {
    const f4 zero = {0.f, 0.f, 0.f, 0.f};
    const int c = l & 15, q = l >> 4;
    const bf8 N0 = neg8(S64[0]), N1 = neg8(S64[1]);
#pragma unroll
    for (int u = 0; u < 2; u++) {
      bf8 ur0 = lds128(L_FFT + (u * 2 + 0) * 2048 + l * 16), ur1 = lds128(L_FFT + (u * 2 + 0) * 2048 + 1024 + l * 16);
      bf8 ui0 = lds128(L_FFT + (u * 2 + 1) * 2048 + l * 16), ui1 = lds128(L_FFT + (u * 2 + 1) * 2048 + 1024 + l * 16);
      f4 a = mfma16(C64[0], ur0, zero), b = mfma16(C64[0], ui0, zero);
      a = mfma16(C64[1], ur1, a); b = mfma16(C64[1], ui1, b);
      a = mfma16(N0, ui0, a); b = mfma16(S64[0], ur0, b);
      a = mfma16(N1, ui1, a); b = mfma16(S64[1], ur1, b);
      const unsigned o = WS_Y1 + (k == 224 ? 8 : k * 8192) + (16 * u + c) * 256 + w4 * 64 + q * 16;
      u4 v = asu4(pack44(a, b));
      if (!special) st16s(v, rws, o, 0); else st8s(u2{v[0], v[1]}, rws, o);
    }
  }
};
DI unsigned yflag_off(int k) { return WS_YF + (k == 224 ? 56 * 8 : ((k >> 2) & 3) * 512 + ((k >> 4) * 4 + (k & 3)) * 8); }
DI void arrive_flag(rsrc_t rws, unsigned off, u2 tag, int l) {
  if (l == 0) {
    unsigned old = __hip_atomic_fetch_add((LAS unsigned*)(uintptr_t)L_CNT, 1u, __ATOMIC_RELAXED, __HIP_MEMORY_SCOPE_WORKGROUP);
    if (old == 3) st8s(tag, rws, off);
  }
}

DI void g_wave(const Params& p, int g, int j, int ordb, int wv, int l) {
  const u64 ep = epoch_load(p.ws);
  u2 tag;
  const int m = wv & 1, T = wv >> 1, h = l >> 5, c = l & 31;
  const rsrc_t rws = mkrs(p.ws, WS_END);
  const unsigned short bias = *(const unsigned short*)(p.b1 + j * 512 + wv * 128 + l * 2);
  f16v acc0, acc1;
  float sgn, cs;
  bf8 xres[2];
  const int row = 32 * m + c;
  unsigned xa[4];
#pragma unroll
  for (int v = 0; v < 4; v++) xa[v] = row * 128 + (((2 * v + h) ^ ((row >> 1) & 7)) << 4);
  {
    bf8 F[14];
    int k = 32 * j + c;
    float ph = T ? 0.25f : 0.f;
    if (k == 0 && T) { k = 224; ph = 0.f; }
    sgn = (k & 1) ? -1.f : 1.f;
    if (ordb && (k & 1)) ph += 0.5f;
    const float th = (float)k * (1.0f / 448.0f);
    gen14<false>(F, __builtin_fmaf((float)(8 * h), th, ph), th, 16.f * th);
#pragma unroll
    for (int i = 0; i < 16; i++) acc0[i] = 0.f;
    const bf8 ones = asbf8(u4{0x3f803f80u, 0x3f803f80u, 0x3f803f80u, 0x3f803f80u});
#pragma unroll
    for (int n = 0; n < 14; n++) acc0 = mfma32(ones, F[n], acc0);
    cs = (k & 1) ? 0.f : 2.f * acc0[0];
    *(LAS float*)(uintptr_t)(L_B1 + wv * 256 + l * 4) = __uint_as_float((unsigned)bias << 16);
    if (wv == 0 && l == 0) *(LAS unsigned*)(uintptr_t)L_CNT = 0u;
    unsigned ad[2][4];
#pragma unroll
    for (int v = 0; v < 4; v++) {
      unsigned lo = xa[v], hi = xa[v ^ 2] + (v < 2 ? 3 * 8192 : 4 * 8192);
      ad[0][v] = ordb ? hi : lo; ad[1][v] = ordb ? lo : hi;
    }
#pragma unroll
    for (int i = 0; i < 16; i++) { acc0[i] = 0.f; acc1[i] = 0.f; }
    tag = epoch_tag(ep);
    STAMP(0, 1);
#pragma unroll
    for (int b = 0; b < 7; b++) {
      const int p0 = b == 0 ? 0 : 4 * b - 2, p1 = b == 6 ? 28 : 4 * b + 2;
      WAITL0();
      BAR();
      bf8 xf[6];
#pragma unroll
      for (int s = p0; s < p1; s++) { const int n = s % 14; xf[s - p0] = lds128(ad[s / 14][n & 3] + (n >> 2) * 8192); }
      if (b == 6) {
#pragma unroll
        for (int t = 0; t < 2; t++) {
          u2 a = *(const LAS u2*)(uintptr_t)(j * 8192 + row * 128 + (((4 * T + 2 * t) ^ ((row >> 1) & 7)) << 4) + h * 8);
          u2 bq = *(const LAS u2*)(uintptr_t)(j * 8192 + row * 128 + (((4 * T + 2 * t + 1) ^ ((row >> 1) & 7)) << 4) + h * 8);
          xres[t] = asbf8(u4{a[0], a[1], bq[0], bq[1]});
        }
      }
#pragma unroll
      for (int s = p0; s < p1; s++) { if (s < 14) acc0 = mfma32(xf[s - p0], F[s], acc0); else acc1 = mfma32(xf[s - p0], F[s - 14], acc1); }
      SCHED();
    }
  }
  STAMP(0, 2);
  WAITL0();
  BAR();
  STAMP(0, 3);
  {
    const unsigned o = WS_X1 + (32 * j + c) * 8192 + g * 256 + m * 128 + T * 32 + h * 16;
#pragma unroll
    for (int t = 0; t < 2; t++) {
      float v[8];
#pragma unroll
      for (int e = 0; e < 2; e++) {
        f4 mm = ldsf4(L_MT + (32 * m + 16 * t + 8 * e + 4 * h) * 4), rr = ldsf4(L_RT + (32 * m + 16 * t + 8 * e + 4 * h) * 4);
#pragma unroll
        for (int i = 0; i < 4; i++) {
          const int r = 8 * t + 4 * e + i;
          v[4 * e + i] = __builtin_fmaf(mm[i], cs, __builtin_fmaf(sgn, acc1[r], acc0[r])) * rr[i];
        }
      }
      st16s(u4{pk(v[0], v[1]), pk(v[2], v[3]), pk(v[4], v[5]), pk(v[6], v[7])}, rws, o, t * 64);
    }
  }
  const int q = (l >> 2) & 3, pp = l & 3, dh = (l >> 4) & 1;
  const unsigned wsw = (((4 * T + 2 * dh + (pp >> 1)) ^ ((q >> 1) << 2)) << 4) + (pp & 1) * 8;
  auto bias_init = [&](int pair) {
#pragma unroll
    for (int e = 0; e < 4; e++) {
      f4 b = ldsf4(L_B1 + (64 * pair + 32 * T + 8 * e + 4 * h) * 4);
#pragma unroll
      for (int i = 0; i < 4; i++) acc0[4 * e + i] = b[i];
    }
  };
  bias_init(0);
  STAMP(0, 4);
  BAR();
  STAMP(0, 5);
  {
    bf8 X[28];
    const unsigned wa = L_RING + (8 * h + q) * 128 + wsw;
#pragma unroll
    for (int u = 0; u < 8; u++) {
      const unsigned wb = wa + (u % 3) * 32768;
      bf8 W[4];
      auto ldw = [&](int n) {
        if (u < 2) { const int s = 14 * u + n; X[s] = lds128(xa[s & 3] + (s >> 2) * 8192); }
        W[n & 3] = cat(ldstr(wb + n * 2048), ldstr(wb + n * 2048 + 512));
      };
#pragma unroll
      for (int n = 0; n < 3; n++) ldw(n);
#pragma unroll
      for (int n = 0; n < 14; n++) {
        if (n + 3 < 14) ldw(n + 3);
        acc0 = mfma32(W[n & 3], X[14 * (u & 1) + n], acc0);
        SCHED();
      }
      if (u & 1) {
#pragma unroll
        for (int e = 0; e < 4; e++) *(LAS f4*)(uintptr_t)(L_ACC + wv * 4096 + e * 1024 + l * 16) = f4{acc0[4 * e], acc0[4 * e + 1], acc0[4 * e + 2], acc0[4 * e + 3]};
        if (u < 7) bias_init((u + 1) >> 1);
      }
      if (u == 0) WAITV(0);
      WAITL0();
      BAR();
      if (u == 0 && wv == 0) flag(rws, WS_XF + j * 256 + g * 8, tag, l);
      STAMP(0, 6 + u);
    }
  }
  u2 bq[4];
  {
    const rsrc_t rb = mkrs(p.b2, 896);
#pragma unroll
    for (int t = 0; t < 4; t++) asm volatile("buffer_load_dwordx2 %0, %1, %2, 0 offen offset:%3" : "=v"(bq[t]) : "v"(j * 128 + T * 64 + h * 8), "s"(rb), "i"(t * 16) : "memory");
  }
  {
    bf8 H[16];
    const unsigned wa = L_RING + (4 * h + q) * 128 + wsw;
    const unsigned ha = L_H + m * 16384 + l * 16;
    int dst = j;
    unsigned pf[6];
#pragma unroll
    for (int r = 0; r < 7; r++) {
      const unsigned wb = wa + ((8 + r) % 3) * 32768;
      dst = dst == 6 ? 0 : dst + 1;
      bf8 W[4];
      auto ldw = [&](int n) {
        if (r == 0) { if (n == 12) { BAR(); } H[n] = lds128(ha + n * 1024); }
        W[n & 3] = cat(ldstr(wb + n * 2048), ldstr(wb + n * 2048 + 1024));
      };
#pragma unroll
      for (int i = 0; i < 16; i++) acc0[i] = 0.f;
#pragma unroll
      for (int n = 0; n < 3; n++) ldw(n);
#pragma unroll
      for (int n = 0; n < 16; n++) {
        if (n + 3 < 16) ldw(n + 3);
        acc0 = mfma32(W[n & 3], H[n], acc0);
        SCHED();
      }
      if (r < 6) {
        const unsigned o = WS_P + (((g * 7 + dst) * 6 + r) * 4 + wv) * 2048 + l * 16;
        st16s(pack8(acc0, 0), rws, o, 0);
        st16s(pack8(acc0, 8), rws, o, 1024);
        pf[r] = WS_PF + ((g * 7 + dst) * 4 + wv) * 64 + r * 8;
      }
      if (r >= 2) {
        if (r == 2) WAITV(4); else if (r == 3) WAITV(5); else if (r < 6) WAITV(6); else WAITV(4);
        flag(rws, pf[r - 2], tag, l);
      }
      if (r < 6) { WAITL0(); BAR(); }
      STAMP(0, 14 + r);
    }
    WAITV(0);
    flag(rws, pf[5], tag, l);
    STAMP(0, 21);
  }
  bf8 sel[2];
#pragma unroll
  for (int t = 0; t < 2; t++) {
    u4 s;
#pragma unroll
    for (int n = 0; n < 4; n++) {
      const int d0 = 16 * t + 2 * (n & 1) + 8 * (n >> 1) + 4 * h;
      s[n] = c == d0 ? 0x3f80u : (c == d0 + 1 ? 0x3f800000u : 0u);
    }
    sel[t] = asbf8(s);
  }
#pragma unroll
  for (int t = 0; t < 4; t++) TIE(bq[t]);
#pragma unroll
  for (int t = 0; t < 2; t++) {
    acc0 = mfma32(sel[t], xres[t], acc0);
    acc0 = mfma32(sel[t], asbf8(u4{bq[2 * t][0], bq[2 * t][1], bq[2 * t + 1][0], bq[2 * t + 1][1]}), acc0);
  }
  bf8 G[28];
  const int d = 64 * j + 32 * T + c;
  const float th = (float)d * (1.0f / 448.0f);
  gen14<true>(G, (float)(4 * h) * th, th, 8.f * th);
  if (h == 0) { u4 t = asu4(G[0]); t[0] = (t[0] & 0xffffu) | ((d & 1) ? 0xbf800000u : 0x3f800000u); G[0] = asbf8(t); }
#pragma unroll
  for (int n = 0; n < 14; n++) TIE(G[n]);
  const unsigned pfo = WS_PF + ((g * 7 + j) * 4 + wv) * 64;
  const unsigned po = WS_P + ((g * 7 + j) * 24 + wv) * 2048 + l * 16;
  poll(rws, pfo + (l < 5 ? l * 8 : 0), tag);
  STAMP(0, 22);
  u4 pc[6][2];
#pragma unroll
  for (int r = 0; r < 5; r++) { pc[r][0] = ld16s(rws, po + r * 8192, 0); pc[r][1] = ld16s(rws, po + r * 8192, 1024); }
  SCHED();
  gen14<true>(G + 14, __builtin_fmaf((float)(4 * h), th, (float)(d & 3) * 0.25f), th, 8.f * th);
#pragma unroll
  for (int n = 14; n < 28; n++) TIE(G[n]);
  poll(rws, pfo + 40, tag);
  STAMP(0, 23);
  pc[5][0] = ld16s(rws, po + 5 * 8192, 0); pc[5][1] = ld16s(rws, po + 5 * 8192, 1024);
#pragma unroll
  for (int r = 0; r < 5; r++) {
    TIE(pc[r][0]); TIE(pc[r][1]);
    acc0 = mfma32(sel[0], asbf8(pc[r][0]), acc0); acc0 = mfma32(sel[1], asbf8(pc[r][1]), acc0);
  }
  const unsigned ya = L_Y + (4 * h + (q >> 1)) * 256 + (((8 * m + 4 * dh + pp) ^ ((q >> 1) << 3)) << 4) + (q & 1) * 8;
#pragma unroll
  for (int e = 0; e < 4; e++) {
    const int s0 = e < 2 ? 8 * e : 6 * e + 4, s1 = e < 2 ? s0 + 8 : s0 + 6;
    BAR();
    bf8 y[8];
#pragma unroll
    for (int s = s0; s < s1; s++) y[s - s0] = cat(ldstr(ya + s * 2048), ldstr(ya + s * 2048 + 512));
#pragma unroll
    for (int s = s0; s < s1; s++) acc0 = mfma32(G[s], y[s - s0], acc0);
    SCHED();
    STAMP(0, 24 + e);
  }
  asm volatile("s_waitcnt vmcnt(0)" : "+v"(pc[5][0]), "+v"(pc[5][1]) :: "memory");
  acc0 = mfma32(sel[0], asbf8(pc[5][0]), acc0); acc0 = mfma32(sel[1], asbf8(pc[5][1]), acc0);
  char* op = p.out + (size_t)(32 * row + g) * 896 + j * 128 + T * 64 + h * 16;
#pragma unroll
  for (int t = 0; t < 2; t++) {
    u4 v = pack8(acc0, 8 * t);
    u2 a = __builtin_amdgcn_permlane32_swap(v[0], v[2], false, false), b = __builtin_amdgcn_permlane32_swap(v[1], v[3], false, false);
    *(u4*)(op + t * 32) = u4{a[0], b[0], a[1], b[1]};
  }
  if (blockIdx.x == 0 && wv == 0) flag(rws, 0, tag, l);
  STAMP(0, 28);
  STAMP_DUMP(0, 0);
}

DI float red8(float v) {
  v += __builtin_bit_cast(float, __builtin_amdgcn_update_dpp(0, __builtin_bit_cast(int, v), 0xB1, 0xf, 0xf, true));
  v += __builtin_bit_cast(float, __builtin_amdgcn_update_dpp(0, __builtin_bit_cast(int, v), 0x4E, 0xf, 0xf, true));
  v += __builtin_bit_cast(float, __builtin_amdgcn_update_dpp(0, __builtin_bit_cast(int, v), 0x141, 0xf, 0xf, true));
  return v;
}
DI void a_wave(const Params& p, int g, int j, int ordb, int wv, int l) {
  const int a = wv - 4;
  const rsrc_t rx = mkrs(p.x, 2048 * 896), r1 = mkrs(p.w1, 448 * 3584), r2 = mkrs(p.w2, 1792 * 896), rws = mkrs(p.ws, WS_END);
  unsigned xo[2];
#pragma unroll
  for (int e = 0; e < 2; e++) { const int row = 16 * a + 8 * e + (l >> 3); xo[e] = (32 * row + g) * 896 + (((l & 7) ^ ((row >> 1) & 7)) << 4); }
  int ch[7];
#pragma unroll
  for (int i = 0; i < 7; i++) ch[i] = ordb ? (i + 3) % 7 : i;
  auto dmax = [&](int i) { dma16(rx, xo[0], ch[i] * 128, ch[i] * 8192 + a * 2048); dma16(rx, xo[1], ch[i] * 128, ch[i] * 8192 + a * 2048 + 1024); };
#pragma unroll
  for (int i = 0; i < 5; i++) dmax(i);
  const u64 ep = epoch_load(p.ws);
  SCHED();
  const unsigned swz = ((l & 7) ^ (((l >> 4) & 1) << 2)) << 4;
  const unsigned o1 = (8 * a + (l >> 3)) * 3584 + j * 512 + swz, o2 = (256 * j + 8 * a + (l >> 3)) * 896 + swz;
  const unsigned ring = L_RING + a * 1024;
  auto dmaw = [&](int v, int n) {
    if (v < 8) dma16(r1, o1, (v >> 1) * 128 + (224 * (v & 1) + 32 * n) * 3584, ring + (v % 3) * 32768 + n * 4096);
    else { int dst = j + v - 7; dst = dst >= 7 ? dst - 7 : dst; dma16(r2, o2, dst * 128 + n * 32 * 896, ring + (v % 3) * 32768 + n * 4096); }
  };
  const int k = 32 * j + g;
  const bool special = k == 0;
  Fft ft;
  ft.consts(a, l, special);
  ft.pin();
  SCHED();
  STAMP(4, 33);
  dmax(5); dmax(6);
  const u2 tag = epoch_tag(ep);
  f2 xv[7][2][4];
  f2 sm[2] = {{0.f, 0.f}, {0.f, 0.f}}, sq[2] = {{0.f, 0.f}, {0.f, 0.f}};
  const unsigned xl = a * 2048 + l * 16;
#pragma unroll
  for (int i = 0; i < 7; i++) {
    if (i == 0) WAITV(12); else if (i == 1) WAITV(10); else WAITV(8);
    BAR();
    if (i == 0) STAMP(4, 34);
    if (i == 6) STAMP(4, 35);
    u4 t[2];
#pragma unroll
    for (int e = 0; e < 2; e++) t[e] = asu4(lds128(xl + ch[i] * 8192 + e * 1024));
    if (i >= 2) { dmaw((2 * i - 4) / 7, (2 * i - 4) % 7); dmaw((2 * i - 3) / 7, (2 * i - 3) % 7); }
#pragma unroll
    for (int e = 0; e < 2; e++)
#pragma unroll
      for (int n = 0; n < 4; n++) {
        f2 v = {bflo(t[e][n]), bfhi(t[e][n])};
        xv[i][e][n] = v; sm[e] += v; sq[e] = __builtin_elementwise_fma(v, v, sq[e]);
      }
    SCHED();
  }
  f2 sc[2], of[2];
#pragma unroll
  for (int e = 0; e < 2; e++) {
    const float mean = red8(sm[e][0] + sm[e][1]) * (1.0f / 448.0f), ssq = red8(sq[e][0] + sq[e][1]) * (1.0f / 448.0f);
    const float var = __builtin_fmaxf(__builtin_fmaf(-mean, mean, ssq), 0.f);
    const float sd = rbf(__builtin_amdgcn_sqrtf(rbf(rbf(var) + 1e-5f)));
    const float r = __builtin_amdgcn_rcpf(sd), nm = -rbf(mean);
    *(LAS float*)(uintptr_t)(L_MT + (16 * a + 8 * e + (l >> 3)) * 4) = nm;
    *(LAS float*)(uintptr_t)(L_RT + (16 * a + 8 * e + (l >> 3)) * 4) = r;
    sc[e] = f2{r, r}; of[e] = f2{nm * r, nm * r};
  }
  WAITL0();
  BAR();
#pragma unroll
  for (int i = 0; i < 7; i++) {
#pragma unroll
    for (int e = 0; e < 2; e++) {
      u4 t;
#pragma unroll
      for (int n = 0; n < 4; n++) t[n] = pk2(__builtin_elementwise_fma(xv[i][e][n], sc[e], of[e]));
      *(LAS u4*)(uintptr_t)(xl + ch[i] * 8192 + e * 1024) = t;
      const int n = 10 + 2 * i + e - 3;
      if (n >= 10) dmaw(n / 7, n % 7);
      SCHED();
    }
  }
  STAMP(4, 36);
  WAITVL(14);
  BAR();
  WAITV(7);
  BAR();
  f4 av[4];
  u2 fl;
  const unsigned xfo = l < 32 ? WS_XF + j * 256 + l * 8 : WS_WF + (l - 32) * 8;
  const unsigned hw = L_H + (a & 1) * 16384 + (a >> 1) * 2048 + l * 16;
#pragma unroll
  for (int v = 1; v < 14; v++) {
    const int nd = v + 2 < 8 ? 7 : (v + 2 < 15 ? 8 : 0);
    if (v == POLL_ROUND) fl = ld8s(rws, xfo);
    if (v == POLL_ROUND + 1) ft.loads(rws, k, a, l);
    if (v <= 8 && !(v & 1)) {
#pragma unroll
      for (int e = 0; e < 4; e++) av[e] = ldsf4(L_ACC + a * 4096 + e * 1024 + l * 16);
    }
#pragma unroll
    for (int n = 0; n < 3; n++) if (n < nd) dmaw(v + 2, n);
    SCHED();
    if (v >= 2 && v <= 8) *(LAS u4*)(uintptr_t)(hw + ((v - 2) >> 1) * 4096 + (v & 1) * 1024) = gelu8(av[2 * (v & 1)], av[2 * (v & 1) + 1]);
    if (v == 9) ft.stageA(special);
    if (v == 10) ft.stageD(rws, k, a, l, special);
    SCHED();
#pragma unroll
    for (int n = 3; n < 5; n++) if (n < nd) dmaw(v + 2, n);
    SCHED();
    if (v == 8) {
      *(LAS u4*)(uintptr_t)(hw + 3 * 4096 + 1024) = gelu8(av[2], av[3]);
      WAITL0();
      BAR();
    }
    if (v == 9) { ft.stageB(); SCHED(); dmaw(v + 2, 5); SCHED(); ft.stageC(a, l); SCHED(); }
#pragma unroll
    for (int n = (v == 9 ? 6 : 5); n < 8; n++) if (n < nd) dmaw(v + 2, n);
    if (v == 10) WAITVL(10); else WAITVL(nd);
    if (v == POLL_ROUND) { TIE(fl); if (!match(fl, tag)) poll(rws, xfo, tag); }
    if (v == POLL_ROUND + 1) ft.arrived();
    if (v == 11) arrive_flag(rws, yflag_off(k), tag, l);
    STAMP(4, 36 + v);
    BAR();
  }
  poll(rws, WS_YF + a * 512 + (l < 56 || (l == 56 && a == 0) ? l * 8 : 0), tag);
  STAMP(4, 50);
  u4 y[14];
  const unsigned yo = WS_Y1 + (4 * a + (l >> 4)) * 8192 + g * 256 + (l & 15) * 16;
#pragma unroll
  for (int n = 0; n < 14; n++) y[n] = ld16s(rws, yo + n * 131072, 0);
  const unsigned yw = L_Y + (4 * a + (l >> 4)) * 256 + (((l & 15) ^ (((l >> 4) & 1) << 3)) << 4);
#pragma unroll
  for (int n = 0; n < 14; n++) {
    asm volatile("s_waitcnt vmcnt(%1)" : "+v"(y[n]) : "i"(13 - n) : "memory");
    *(LAS u4*)(uintptr_t)(yw + n * 4096) = y[n];
    if (n == 3 || n == 7 || n == 10 || n == 13) { WAITL0(); BAR(); }
  }
  STAMP(4, 51);
  STAMP_DUMP(4, 32);
}

DI void helper(const Params& p, int id, int wv, int l, u64 pc) {
  const int tid = threadIdx.x;
  const rsrc_t rws = mkrs(p.ws, WS_END);
  unsigned d0, d1, d2;
  {
    const rsrc_t rc = mkrs((const void*)pc, CODE_LINES * 128), r1 = mkrs(p.w1, 448 * 3584);
    asm volatile("buffer_load_dword %0, %1, %2, 0 offen" : "=v"(d0) : "v"(tid * 128), "s"(rc) : "memory");
    const int e = (id * 2 + (wv & 1)) * 49 + l;
    asm volatile("buffer_load_dword %0, %1, %2, 0 offen" : "=v"(d1) : "v"(l < 49 ? e * 512 + (wv >> 1) * 128 : 0x7fffffff), "s"(r1) : "memory");
  }
  const int w4 = id >> 3, f20 = (id & 7) * 4;
  {
    const rsrc_t rw = mkrs(wv >= 4 ? p.wi : p.wr, 2048 * 450);
    const unsigned so = (16 * w4 + 64 * (f20 + (wv & 3))) * 450;
#pragma unroll
    for (int n = 0; n < 8; n++) {
      const unsigned vo = (n < 7 || l < 2) ? n * 1024 + l * 16 : 0x7fffffffu;
      asm volatile("s_mov_b32 m0, %3\n\ts_nop 0\n\tbuffer_load_dwordx4 %0, %1, %2 offen nt lds" :: "v"(vo), "s"(rw), "s"(so), "s"(16384 + wv * 8192 + n * 1024) : "memory");
    }
    const rsrc_t r2 = mkrs(p.w2, 1792 * 896);
    asm volatile("buffer_load_dword %0, %1, %2, 0 offen" : "=v"(d2) : "v"(l < 49 ? ((id * 8 + wv) * 49 + l) * 128 : 0x7fffffff), "s"(r2) : "memory");
  }
  if (tid == 0) *(LAS unsigned*)(uintptr_t)L_CNT = 0u;
  const u2 tag = epoch_tag(epoch_load(p.ws));
  STAMP(0, 1);
  WAITVL(1);
  BAR();
  STAMP(0, 29);
  for (int e = tid; e < 3600; e += 512) {
    const int k = e >> 4, c = e & 15;
    u4 v;
#pragma unroll
    for (int i = 0; i < 4; i++) {
      unsigned a = *(const LAS unsigned short*)(uintptr_t)(16384 + i * 8192 + c * 450 + k * 2);
      unsigned b = *(const LAS unsigned short*)(uintptr_t)(16384 + 32768 + i * 8192 + c * 450 + k * 2);
      v[i] = a | (b << 16);
    }
    st16s(v, rws, WS_WT + k * 8192 + id * 256 + c * 16, 0);
  }
  STAMP(0, 30);
  asm volatile("s_waitcnt vmcnt(0)" : "+v"(d0), "+v"(d1), "+v"(d2) :: "memory");
  BAR();
  if (wv == 0) flag(rws, WS_WF + id * 8, tag, l);
  STAMP(0, 31);
  STAMP_DUMP(0, 0);
  if (id != 0 || wv >= 4) return;
  Fft ft;
  ft.consts(wv, l, true);
  ft.pin();
  poll(rws, l < 32 ? WS_XF + l * 8 : WS_WF + (l - 32) * 8, tag);
  ft.loads(rws, 224, wv, l);
  WAITV(0);
  ft.arrived();
  ft.stageA(true); ft.stageB(); ft.stageC(wv, l);
  WAITL0();
  BAR();
  ft.stageD(rws, 224, wv, l, true);
  WAITV(0);
  arrive_flag(rws, yflag_off(224), tag, l);
}

__global__ __launch_bounds__(512, 2) void k_fused(Params p) {
  u64 pc; asm volatile("s_getpc_b64 %0" : "=s"(pc));
  const int b = blockIdx.x, xc = b & 7, i = b >> 3;
  const int l = threadIdx.x & 63, wv = __builtin_amdgcn_readfirstlane(threadIdx.x >> 6);
  STAMP_INIT();
  STAMP(0, 0);
  STAMP(4, 32);
  if (i >= 28) { helper(p, xc * 4 + (i - 28), wv, l, pc & ~127ull); return; }
  const int j = i < 24 ? (xc & 1) * 4 + i % 3 : 3;
  const int g = (xc >> 1) * 8 + (i < 24 ? i / 3 : (xc & 1) * 4 + i - 24);
  const int ordb = j & 1;
  if (wv < 4) g_wave(p, g, j, ordb, wv, l); else a_wave(p, g, j, ordb, wv, l);
}

extern "C" void kernel_launch(void* const* d_in, const int* in_sizes, int n_in, void* d_out, int out_size, void* d_ws, size_t ws_size, hipStream_t stream) {
  Params p;
  p.x = (const char*)d_in[0]; p.wr = (const char*)d_in[1]; p.wi = (const char*)d_in[2]; p.w1 = (const char*)d_in[4];
  p.b1 = (const char*)d_in[5]; p.w2 = (const char*)d_in[6]; p.b2 = (const char*)d_in[7]; p.out = (char*)d_out; p.ws = (char*)d_ws;
  (void)hipFuncSetAttribute((const void*)k_fused, hipFuncAttributeMaxDynamicSharedMemorySize, L_TOTAL);
  k_fused<<<256, 512, L_TOTAL, stream>>>(p);
}
```

```cpp
#include <hip/hip_runtime.h>
#include <stdint.h>

typedef __bf16 bf2 __attribute__((ext_vector_type(2)));
typedef __bf16 bf4 __attribute__((ext_vector_type(4)));
typedef __bf16 bf8 __attribute__((ext_vector_type(8)));
typedef float f2 __attribute__((ext_vector_type(2)));
typedef float f4 __attribute__((ext_vector_type(4)));
typedef float f16v __attribute__((ext_vector_type(16)));
typedef unsigned u2 __attribute__((ext_vector_type(2)));
typedef unsigned u4 __attribute__((ext_vector_type(4)));
typedef unsigned long long u64;
#define LAS __attribute__((address_space(3)))
#define DI __device__ __forceinline__
typedef __amdgpu_buffer_rsrc_t rsrc_t;

constexpr int WS_XF = 4096;
constexpr int WS_WF = WS_XF + 2048;
constexpr int WS_YF = 8192;
constexpr int WS_PF = 16384;
constexpr int WS_WT = 131072;
constexpr int WS_X1 = WS_WT + 232 * 8192;
constexpr int WS_Y1 = WS_X1 + 224 * 8192;
constexpr int WS_P = WS_Y1 + 224 * 8192;
constexpr int WS_END = WS_P + 32 * 7 * 6 * 8192;
constexpr int SC1 = 16;
constexpr unsigned L_FFT = 0, L_ACC = 8192, L_H = 24576, L_Y = 8192, L_RING = 57344;
constexpr unsigned L_B1 = 155648, L_MT = L_B1 + 1024, L_RT = L_MT + 256, L_CNT = L_RT + 256, L_ST = L_CNT + 256;
constexpr unsigned L_TOTAL = 163840;
constexpr int SPIN_CAP = 20000;
#define POLL_ROUND 6
#define CODE_LINES 280

struct Params {
  const char* x; const char* wr; const char* wi; const char* w1; const char* b1; const char* w2; const char* b2;
  char* out; char* ws;
};

DI unsigned pk(float a, float b) { f2 v = {a, b}; bf2 r = __builtin_convertvector(v, bf2); return __builtin_bit_cast(unsigned, r); }
DI unsigned pk2(f2 v) { bf2 r = __builtin_convertvector(v, bf2); return __builtin_bit_cast(unsigned, r); }
DI float rbf(float a) { return __uint_as_float(pk(a, a) << 16); }
DI float bflo(unsigned u) { return __uint_as_float(u << 16); }
DI float bfhi(unsigned u) { return __uint_as_float(u & 0xffff0000u); }
DI bf8 asbf8(u4 v) { return __builtin_bit_cast(bf8, v); }
DI u4 asu4(bf8 v) { return __builtin_bit_cast(u4, v); }
DI f16v mfma32(bf8 a, bf8 b, f16v c) { return __builtin_amdgcn_mfma_f32_32x32x16_bf16(a, b, c, 0, 0, 0); }
DI f4 mfma16(bf8 a, bf8 b, f4 c) { return __builtin_amdgcn_mfma_f32_16x16x32_bf16(a, b, c, 0, 0, 0); }
DI bf8 lds128(unsigned a) { return *(const LAS bf8*)(uintptr_t)a; }
DI f4 ldsf4(unsigned a) { return *(const LAS f4*)(uintptr_t)a; }
DI bf4 ldstr(unsigned a) { return __builtin_amdgcn_ds_read_tr16_b64_v4bf16((LAS bf4*)(uintptr_t)a); }
DI bf8 cat(bf4 a, bf4 b) { return __builtin_shufflevector(a, b, 0, 1, 2, 3, 4, 5, 6, 7); }
DI float cosr(float rev) { return __builtin_amdgcn_cosf(rev); }
DI float sinr(float rev) { return __builtin_amdgcn_sinf(rev); }
DI rsrc_t mkrs(const void* p, int n) { return __builtin_amdgcn_make_buffer_rsrc((void*)p, 0, n, 0x00020000); }
DI void dma16(rsrc_t rs, unsigned voff, unsigned soff, unsigned lds) {
  asm volatile("s_mov_b32 m0, %3\n\ts_nop 0\n\tbuffer_load_dwordx4 %0, %1, %2 offen lds" :: "v"(voff), "s"(rs), "s"(soff), "s"(lds) : "memory");
}
DI u4 ld16s(rsrc_t rs, unsigned voff, int imm) {
  u4 t; asm volatile("buffer_load_dwordx4 %0, %1, %2, 0 offen offset:%3 sc1" : "=v"(t) : "v"(voff), "s"(rs), "i"(imm) : "memory"); return t;
}
DI u2 ld8s(rsrc_t rs, unsigned voff) {
  u2 t; asm volatile("buffer_load_dwordx2 %0, %1, %2, 0 offen sc1" : "=v"(t) : "v"(voff), "s"(rs) : "memory"); return t;
}
DI void st16s(u4 v, rsrc_t rs, unsigned voff, int imm) { asm volatile("buffer_store_dwordx4 %0, %1, %2, 0 offen offset:%3 sc1\n\ts_nop 1" :: "v"(v), "v"(voff), "s"(rs), "i"(imm) : "memory"); }
DI void st8s(u2 v, rsrc_t rs, unsigned voff) { asm volatile("buffer_store_dwordx2 %0, %1, %2, 0 offen sc1" :: "v"(v), "v"(voff), "s"(rs) : "memory"); }
#define WAITV(n) asm volatile("s_waitcnt vmcnt(%0)" :: "i"(n) : "memory")
#define WAITVL(n) asm volatile("s_waitcnt vmcnt(%0) lgkmcnt(0)" :: "i"(n) : "memory")
#define WAITL0() asm volatile("s_waitcnt lgkmcnt(0)" ::: "memory")
#define TIE(x) asm volatile("" : "+v"(x))
#define BAR() do { asm volatile("" ::: "memory"); __builtin_amdgcn_s_barrier(); asm volatile("" ::: "memory"); } while (0)
#define SCHED() __builtin_amdgcn_sched_barrier(0)

DI bool match(u2 v, u2 tag) { return __builtin_amdgcn_ballot_w64(v[0] != tag[0] || v[1] != tag[1]) == 0; }
DI void poll(rsrc_t rs, unsigned voff, u2 tag) {
#pragma unroll 1
  for (int n = 0; n < SPIN_CAP; n++) {
    u2 v = ld8s(rs, voff);
    asm volatile("s_waitcnt vmcnt(0)" : "+v"(v) :: "memory");
    if (match(v, tag)) break;
    __builtin_amdgcn_s_sleep(1);
  }
}
DI u64 epoch_load(const char* ws) { u64 e; asm volatile("s_load_dwordx2 %0, %1, 0x0" : "=s"(e) : "s"(ws) : "memory"); return e; }
DI u2 epoch_tag(u64 e) {
  asm volatile("s_waitcnt lgkmcnt(0)" : "+s"(e) :: "memory");
  e = e * 6364136223846793005ull + 1442695040888963407ull;
  return u2{(unsigned)e, (unsigned)(e >> 32)};
}
DI void flag(rsrc_t rs, unsigned off, u2 tag, int l) { if (l == 0) st8s(tag, rs, off); }

DI u4 gelu8(f4 a, f4 b) {
  float g[8];
#pragma unroll
  for (int i = 0; i < 8; i++) {
    float u = i < 4 ? a[i & 3] : b[i & 3];
    float z = u * __builtin_fmaf(u * u, -0.10294324f, -2.3022082f);
    g[i] = u * __builtin_amdgcn_rcpf(1.0f + __builtin_amdgcn_exp2f(z));
  }
  return u4{pk(g[0], g[1]), pk(g[2], g[3]), pk(g[4], g[5]), pk(g[6], g[7])};
}
template <bool PAIRS> DI void gen14(bf8* out, float a0, float th, float dn) {
  f2 u[4], v[4];
#pragma unroll
  for (int i = 0; i < 8; i++) {
    float e = PAIRS ? __builtin_fmaf((float)(i >> 1), th, a0 + ((i & 1) ? 0.25f : 0.f)) : __builtin_fmaf((float)i, th, a0);
    u[i >> 1][i & 1] = cosr(e); v[i >> 1][i & 1] = cosr(e + dn);
  }
  const float c = 2.f * cosr(dn);
  const f2 c2 = {c, c};
#pragma unroll
  for (int n = 0; n < 14; n += 2) {
    out[n] = asbf8(u4{pk2(u[0]), pk2(u[1]), pk2(u[2]), pk2(u[3])});
    out[n + 1] = asbf8(u4{pk2(v[0]), pk2(v[1]), pk2(v[2]), pk2(v[3])});
    if (n < 12) {
#pragma unroll
      for (int i = 0; i < 4; i++) u[i] = __builtin_elementwise_fma(c2, v[i], -u[i]);
#pragma unroll
      for (int i = 0; i < 4; i++) v[i] = __builtin_elementwise_fma(c2, u[i], -v[i]);
    }
  }
}
DI u4 pack8(const f16v& a, int o) { return u4{pk(a[o], a[o + 1]), pk(a[o + 2], a[o + 3]), pk(a[o + 4], a[o + 5]), pk(a[o + 6], a[o + 7])}; }

DI bf8 gen8(float a0, float da_lo, float da_hi) {
  float v[8];
#pragma unroll
  for (int j = 0; j < 8; j++) v[j] = cosr(a0 + (float)(j & 3) * da_lo + (float)(j >> 2) * da_hi);
  return asbf8(u4{pk(v[0], v[1]), pk(v[2], v[3]), pk(v[4], v[5]), pk(v[6], v[7])});
}
DI bf8 neg8(bf8 a) { u4 v = asu4(a); return asbf8(v ^ 0x80008000u); }
DI bf8 pack44(f4 a, f4 b) { return asbf8(u4{pk(a[0], a[1]), pk(a[2], a[3]), pk(b[0], b[1]), pk(b[2], b[3])}); }
struct Fft {
  bf8 C64[2], S64[2], C32[2], S32[2];
  float tc[2][4], ts[2][4], ic[2][4], is[2][4];
  u4 z[2][2][2], wt[2];
  bf8 pr, pi;
  DI void consts(int w4, int l, bool special) {
    const int c = l & 15, q = l >> 4;
    const float f1 = (float)(16 * w4 + c);
#pragma unroll
    for (int e = 0; e < 2; e++) {
      float d = f1 * (1.0f / 64.0f);
      float a0 = d * (float)(32 * e + 16 * (q >> 1) + 4 * (q & 1));
      C64[e] = gen8(a0, d, 8.f * d); S64[e] = gen8(a0 - 0.25f, d, 8.f * d);
      float d2 = (float)(16 * e + c) * (1.0f / 32.0f);
      float b0 = d2 * (float)(4 * q);
      C32[e] = gen8(b0, d2, 16.f * d2); S32[e] = gen8(b0 - 0.25f, d2, 16.f * d2);
    }
    const float scale = (special ? 1.0f : 2.0f) / (2048.0f * 448.0f);
#pragma unroll
    for (int u = 0; u < 2; u++)
#pragma unroll
      for (int i = 0; i < 4; i++) {
        float a = (float)(16 * u + 4 * q + i) * f1 * (1.0f / 2048.0f);
        tc[u][i] = cosr(a); ts[u][i] = sinr(a);
        float b = (float)((16 * u + c) * (16 * w4 + 4 * q + i)) * (1.0f / 2048.0f);
        ic[u][i] = cosr(b) * scale; is[u][i] = sinr(b) * scale;
      }
  }
  DI void pin() {
#pragma unroll
    for (int e = 0; e < 2; e++) {
      TIE(C64[e]); TIE(S64[e]); TIE(C32[e]); TIE(S32[e]);
#pragma unroll
      for (int i = 0; i < 4; i++) { TIE(tc[e][i]); TIE(ts[e][i]); TIE(ic[e][i]); TIE(is[e][i]); }
    }
  }
  DI void loads(rsrc_t rws, int k, int w4, int l) {
    const int c = l & 15, q = l >> 4;
    const unsigned o = WS_X1 + (k == 224 ? 32 : k * 8192) + c * 256 + (q >> 1) * 64 + (q & 1) * 16, o2 = o + 4096;
#pragma unroll
    for (int hf = 0; hf < 2; hf++) {
      z[0][0][hf] = ld16s(rws, o, hf * 128); z[0][1][hf] = ld16s(rws, o, hf * 128 + 32);
      z[1][0][hf] = ld16s(rws, o2, hf * 128); z[1][1][hf] = ld16s(rws, o2, hf * 128 + 32);
    }
    const unsigned ow = WS_WT + k * 8192 + w4 * 2048 + l * 16;
    wt[0] = ld16s(rws, ow, 0); wt[1] = ld16s(rws, ow, 1024);
  }
  DI void arrived() {
#pragma unroll
    for (int u = 0; u < 2; u++) { TIE(z[u][0][0]); TIE(z[u][0][1]); TIE(z[u][1][0]); TIE(z[u][1][1]); TIE(wt[u]); }
  }
  DI void stageA(bool special) {
    const f4 zero = {0.f, 0.f, 0.f, 0.f};
    const bf8 N0 = neg8(S64[0]), N1 = neg8(S64[1]);
    f4 re[2], im[2];
#pragma unroll
    for (int u = 0; u < 2; u++) {
      const unsigned msk = special ? 0u : ~0u;
      bf8 r0 = asbf8(z[u][0][0]), r1 = asbf8(z[u][0][1]), i0 = asbf8(z[u][1][0] & msk), i1 = asbf8(z[u][1][1] & msk);
      f4 a = mfma16(r0, C64[0], zero), b = mfma16(i0, C64[0], zero);
      a = mfma16(r1, C64[1], a); b = mfma16(i1, C64[1], b);
      a = mfma16(i0, S64[0], a); b = mfma16(r0, N0, b);
      a = mfma16(i1, S64[1], a); b = mfma16(r1, N1, b);
#pragma unroll
      for (int i = 0; i < 4; i++) { re[u][i] = a[i] * tc[u][i] + b[i] * ts[u][i]; im[u][i] = b[i] * tc[u][i] - a[i] * ts[u][i]; }
    }
    pr = pack44(re[0], re[1]); pi = pack44(im[0], im[1]);
  }
  DI void stageB() {
    const f4 zero = {0.f, 0.f, 0.f, 0.f};
    const bf8 nr = neg8(pr);
    f4 re[2], im[2];
#pragma unroll
    for (int u = 0; u < 2; u++) {
      f4 a = mfma16(C32[u], pr, zero), b = mfma16(C32[u], pi, zero);
      a = mfma16(S32[u], pi, a); b = mfma16(S32[u], nr, b);
#pragma unroll
      for (int i = 0; i < 4; i++) { float wr = bflo(wt[u][i]), wi = bfhi(wt[u][i]); re[u][i] = a[i] * wr - b[i] * wi; im[u][i] = a[i] * wi + b[i] * wr; }
    }
    pr = pack44(re[0], re[1]); pi = pack44(im[0], im[1]);
  }
  DI void stageC(int w4, int l) {
    const f4 zero = {0.f, 0.f, 0.f, 0.f};
    const int c = l & 15, q = l >> 4;
    const bf8 ni = neg8(pi);
    const unsigned wo = L_FFT + (w4 >> 1) * 1024 + ((2 * (w4 & 1) + (q & 1)) * 16 + c) * 16 + (q >> 1) * 8;
#pragma unroll
    for (int u = 0; u < 2; u++) {
      f4 a = mfma16(pr, C32[u], zero), b = mfma16(pi, C32[u], zero);
      a = mfma16(ni, S32[u], a); b = mfma16(pr, S32[u], b);
      float xr[4], xi[4];
#pragma unroll
      for (int i = 0; i < 4; i++) { xr[i] = a[i] * ic[u][i] - b[i] * is[u][i]; xi[i] = b[i] * ic[u][i] + a[i] * is[u][i]; }
      *(LAS u2*)(uintptr_t)(wo + (u * 2 + 0) * 2048) = u2{pk(xr[0], xr[1]), pk(xr[2], xr[3])};
      *(LAS u2*)(uintptr_t)(wo + (u * 2 + 1) * 2048) = u2{pk(xi[0], xi[1]), pk(xi[2], xi[3])};
    }
  }
  DI void stageD(rsrc_t rws, int k, int w4, int l, bool special) {
    const f4 zero = {0.f, 0.f, 0.f, 0.f};
    const int c = l & 15, q = l >> 4;
    const bf8 N0 = neg8(S64[0]), N1 = neg8(S64[1]);
#pragma unroll
    for (int u = 0; u < 2; u++) {
      bf8 ur0 = lds128(L_FFT + (u * 2 + 0) * 2048 + l * 16), ur1 = lds128(L_FFT + (u * 2 + 0) * 2048 + 1024 + l * 16);
      bf8 ui0 = lds128(L_FFT + (u * 2 + 1) * 2048 + l * 16), ui1 = lds128(L_FFT + (u * 2 + 1) * 2048 + 1024 + l * 16);
      f4 a = mfma16(C64[0], ur0, zero), b = mfma16(C64[0], ui0, zero);
      a = mfma16(C64[1], ur1, a); b = mfma16(C64[1], ui1, b);
      a = mfma16(N0, ui0, a); b = mfma16(S64[0], ur0, b);
      a = mfma16(N1, ui1, a); b = mfma16(S64[1], ur1, b);
      const unsigned o = WS_Y1 + (k == 224 ? 8 : k * 8192) + (16 * u + c) * 256 + w4 * 64 + q * 16;
      u4 v = asu4(pack44(a, b));
      if (!special) st16s(v, rws, o, 0); else st8s(u2{v[0], v[1]}, rws, o);
    }
  }
};
DI unsigned yflag_off(int k) { return WS_YF + (k == 224 ? 56 * 8 : ((k >> 2) & 3) * 512 + ((k >> 4) * 4 + (k & 3)) * 8); }
DI void arrive_flag(rsrc_t rws, unsigned off, u2 tag, int l) {
  if (l == 0) {
    unsigned old = __hip_atomic_fetch_add((LAS unsigned*)(uintptr_t)L_CNT, 1u, __ATOMIC_RELAXED, __HIP_MEMORY_SCOPE_WORKGROUP);
    if (old == 3) st8s(tag, rws, off);
  }
}

DI void g_wave(const Params& p, int g, int j, int ordb, int wv, int l) {
  const u64 ep = epoch_load(p.ws);
  u2 tag;
  const int m = wv & 1, T = wv >> 1, h = l >> 5, c = l & 31;
  const rsrc_t rws = mkrs(p.ws, WS_END);
  const unsigned short bias = *(const unsigned short*)(p.b1 + j * 512 + wv * 128 + l * 2);
  f16v acc0, acc1;
  float sgn, cs;
  bf8 xres[2];
  const int row = 32 * m + c;
  unsigned xa[4];
#pragma unroll
  for (int v = 0; v < 4; v++) xa[v] = row * 128 + (((2 * v + h) ^ ((row >> 1) & 7)) << 4);
  {
    bf8 F[14];
    int k = 32 * j + c;
    float ph = T ? 0.25f : 0.f;
    if (k == 0 && T) { k = 224; ph = 0.f; }
    sgn = (k & 1) ? -1.f : 1.f;
    if (ordb && (k & 1)) ph += 0.5f;
    const float th = (float)k * (1.0f / 448.0f);
    gen14<false>(F, __builtin_fmaf((float)(8 * h), th, ph), th, 16.f * th);
#pragma unroll
    for (int i = 0; i < 16; i++) acc0[i] = 0.f;
    const bf8 ones = asbf8(u4{0x3f803f80u, 0x3f803f80u, 0x3f803f80u, 0x3f803f80u});
#pragma unroll
    for (int n = 0; n < 14; n++) acc0 = mfma32(ones, F[n], acc0);
    cs = (k & 1) ? 0.f : 2.f * acc0[0];
    *(LAS float*)(uintptr_t)(L_B1 + wv * 256 + l * 4) = __uint_as_float((unsigned)bias << 16);
    if (wv == 0 && l == 0) *(LAS unsigned*)(uintptr_t)L_CNT = 0u;
    unsigned ad[2][4];
#pragma unroll
    for (int v = 0; v < 4; v++) {
      unsigned lo = xa[v], hi = xa[v ^ 2] + (v < 2 ? 3 * 8192 : 4 * 8192);
      ad[0][v] = ordb ? hi : lo; ad[1][v] = ordb ? lo : hi;
    }
#pragma unroll
    for (int i = 0; i < 16; i++) { acc0[i] = 0.f; acc1[i] = 0.f; }
    tag = epoch_tag(ep);
#pragma unroll
    for (int b = 0; b < 7; b++) {
      const int p0 = b == 0 ? 0 : 4 * b - 2, p1 = b == 6 ? 28 : 4 * b + 2;
      WAITL0();
      BAR();
      bf8 xf[6];
#pragma unroll
      for (int s = p0; s < p1; s++) { const int n = s % 14; xf[s - p0] = lds128(ad[s / 14][n & 3] + (n >> 2) * 8192); }
      if (b == 6) {
#pragma unroll
        for (int t = 0; t < 2; t++) {
          u2 a = *(const LAS u2*)(uintptr_t)(j * 8192 + row * 128 + (((4 * T + 2 * t) ^ ((row >> 1) & 7)) << 4) + h * 8);
          u2 bq = *(const LAS u2*)(uintptr_t)(j * 8192 + row * 128 + (((4 * T + 2 * t + 1) ^ ((row >> 1) & 7)) << 4) + h * 8);
          xres[t] = asbf8(u4{a[0], a[1], bq[0], bq[1]});
        }
      }
#pragma unroll
      for (int s = p0; s < p1; s++) { if (s < 14) acc0 = mfma32(xf[s - p0], F[s], acc0); else acc1 = mfma32(xf[s - p0], F[s - 14], acc1); }
      SCHED();
    }
  }
  WAITL0();
  BAR();
  {
    const unsigned o = WS_X1 + (32 * j + c) * 8192 + g * 256 + m * 128 + T * 32 + h * 16;
#pragma unroll
    for (int t = 0; t < 2; t++) {
      float v[8];
#pragma unroll
      for (int e = 0; e < 2; e++) {
        f4 mm = ldsf4(L_MT + (32 * m + 16 * t + 8 * e + 4 * h) * 4), rr = ldsf4(L_RT + (32 * m + 16 * t + 8 * e + 4 * h) * 4);
#pragma unroll
        for (int i = 0; i < 4; i++) {
          const int r = 8 * t + 4 * e + i;
          v[4 * e + i] = __builtin_fmaf(mm[i], cs, __builtin_fmaf(sgn, acc1[r], acc0[r])) * rr[i];
        }
      }
      st16s(u4{pk(v[0], v[1]), pk(v[2], v[3]), pk(v[4], v[5]), pk(v[6], v[7])}, rws, o, t * 64);
    }
  }
  const int q = (l >> 2) & 3, pp = l & 3, dh = (l >> 4) & 1;
  const unsigned wsw = (((4 * T + 2 * dh + (pp >> 1)) ^ ((q >> 1) << 2)) << 4) + (pp & 1) * 8;
  auto bias_init = [&](int pair) {
#pragma unroll
    for (int e = 0; e < 4; e++) {
      f4 b = ldsf4(L_B1 + (64 * pair + 32 * T + 8 * e + 4 * h) * 4);
#pragma unroll
      for (int i = 0; i < 4; i++) acc0[4 * e + i] = b[i];
    }
  };
  bias_init(0);
  BAR();
  {
    bf8 X[28];
    const unsigned wa = L_RING + (8 * h + q) * 128 + wsw;
#pragma unroll
    for (int u = 0; u < 8; u++) {
      const unsigned wb = wa + (u % 3) * 32768;
      bf8 W[4];
      auto ldw = [&](int n) {
        if (u < 2) { const int s = 14 * u + n; X[s] = lds128(xa[s & 3] + (s >> 2) * 8192); }
        W[n & 3] = cat(ldstr(wb + n * 2048), ldstr(wb + n * 2048 + 512));
      };
#pragma unroll
      for (int n = 0; n < 3; n++) ldw(n);
#pragma unroll
      for (int n = 0; n < 14; n++) {
        if (n + 3 < 14) ldw(n + 3);
        acc0 = mfma32(W[n & 3], X[14 * (u & 1) + n], acc0);
        SCHED();
      }
      if (u & 1) {
#pragma unroll
        for (int e = 0; e < 4; e++) *(LAS f4*)(uintptr_t)(L_ACC + wv * 4096 + e * 1024 + l * 16) = f4{acc0[4 * e], acc0[4 * e + 1], acc0[4 * e + 2], acc0[4 * e + 3]};
        if (u < 7) bias_init((u + 1) >> 1);
      }
      if (u == 0) WAITV(0);
      WAITL0();
      BAR();
      if (u == 0 && wv == 0) flag(rws, WS_XF + j * 256 + g * 8, tag, l);
    }
  }
  u2 bq[4];
  {
    const rsrc_t rb = mkrs(p.b2, 896);
#pragma unroll
    for (int t = 0; t < 4; t++) asm volatile("buffer_load_dwordx2 %0, %1, %2, 0 offen offset:%3" : "=v"(bq[t]) : "v"(j * 128 + T * 64 + h * 8), "s"(rb), "i"(t * 16) : "memory");
  }
  {
    bf8 H[16];
    const unsigned wa = L_RING + (4 * h + q) * 128 + wsw;
    const unsigned ha = L_H + m * 16384 + l * 16;
    int dst = j;
    unsigned pf[6];
#pragma unroll
    for (int r = 0; r < 7; r++) {
      const unsigned wb = wa + ((8 + r) % 3) * 32768;
      dst = dst == 6 ? 0 : dst + 1;
      bf8 W[4];
      auto ldw = [&](int n) {
        if (r == 0) { if (n == 12) { BAR(); } H[n] = lds128(ha + n * 1024); }
        W[n & 3] = cat(ldstr(wb + n * 2048), ldstr(wb + n * 2048 + 1024));
      };
#pragma unroll
      for (int i = 0; i < 16; i++) acc0[i] = 0.f;
#pragma unroll
      for (int n = 0; n < 3; n++) ldw(n);
#pragma unroll
      for (int n = 0; n < 16; n++) {
        if (n + 3 < 16) ldw(n + 3);
        acc0 = mfma32(W[n & 3], H[n], acc0);
        SCHED();
      }
      if (r < 6) {
        const unsigned o = WS_P + (((g * 7 + dst) * 6 + r) * 4 + wv) * 2048 + l * 16;
        st16s(pack8(acc0, 0), rws, o, 0);
        st16s(pack8(acc0, 8), rws, o, 1024);
        pf[r] = WS_PF + ((g * 7 + dst) * 4 + wv) * 64 + r * 8;
      }
      if (r >= 2) {
        if (r == 2) WAITV(4); else if (r == 3) WAITV(5); else if (r < 6) WAITV(6); else WAITV(4);
        flag(rws, pf[r - 2], tag, l);
      }
      if (r < 6) { WAITL0(); BAR(); }
    }
    WAITV(0);
    flag(rws, pf[5], tag, l);
  }
  bf8 sel[2];
#pragma unroll
  for (int t = 0; t < 2; t++) {
    u4 s;
#pragma unroll
    for (int n = 0; n < 4; n++) {
      const int d0 = 16 * t + 2 * (n & 1) + 8 * (n >> 1) + 4 * h;
      s[n] = c == d0 ? 0x3f80u : (c == d0 + 1 ? 0x3f800000u : 0u);
    }
    sel[t] = asbf8(s);
  }
#pragma unroll
  for (int t = 0; t < 4; t++) TIE(bq[t]);
#pragma unroll
  for (int t = 0; t < 2; t++) {
    acc0 = mfma32(sel[t], xres[t], acc0);
    acc0 = mfma32(sel[t], asbf8(u4{bq[2 * t][0], bq[2 * t][1], bq[2 * t + 1][0], bq[2 * t + 1][1]}), acc0);
  }
  bf8 G[28];
  const int d = 64 * j + 32 * T + c;
  const float th = (float)d * (1.0f / 448.0f);
  gen14<true>(G, (float)(4 * h) * th, th, 8.f * th);
  if (h == 0) { u4 t = asu4(G[0]); t[0] = (t[0] & 0xffffu) | ((d & 1) ? 0xbf800000u : 0x3f800000u); G[0] = asbf8(t); }
#pragma unroll
  for (int n = 0; n < 14; n++) TIE(G[n]);
  const unsigned pfo = WS_PF + ((g * 7 + j) * 4 + wv) * 64;
  const unsigned po = WS_P + ((g * 7 + j) * 24 + wv) * 2048 + l * 16;
  poll(rws, pfo + (l < 5 ? l * 8 : 0), tag);
  u4 pc[6][2];
#pragma unroll
  for (int r = 0; r < 5; r++) { pc[r][0] = ld16s(rws, po + r * 8192, 0); pc[r][1] = ld16s(rws, po + r * 8192, 1024); }
  SCHED();
  gen14<true>(G + 14, __builtin_fmaf((float)(4 * h), th, (float)(d & 3) * 0.25f), th, 8.f * th);
#pragma unroll
  for (int n = 14; n < 28; n++) TIE(G[n]);
  poll(rws, pfo + 40, tag);
  pc[5][0] = ld16s(rws, po + 5 * 8192, 0); pc[5][1] = ld16s(rws, po + 5 * 8192, 1024);
#pragma unroll
  for (int r = 0; r < 5; r++) {
    TIE(pc[r][0]); TIE(pc[r][1]);
    acc0 = mfma32(sel[0], asbf8(pc[r][0]), acc0); acc0 = mfma32(sel[1], asbf8(pc[r][1]), acc0);
  }
  const unsigned ya = L_Y + (4 * h + (q >> 1)) * 256 + (((8 * m + 4 * dh + pp) ^ ((q >> 1) << 3)) << 4) + (q & 1) * 8;
#pragma unroll
  for (int e = 0; e < 4; e++) {
    const int s0 = e < 2 ? 8 * e : 6 * e + 4, s1 = e < 2 ? s0 + 8 : s0 + 6;
    BAR();
    bf8 y[8];
#pragma unroll
    for (int s = s0; s < s1; s++) y[s - s0] = cat(ldstr(ya + s * 2048), ldstr(ya + s * 2048 + 512));
#pragma unroll
    for (int s = s0; s < s1; s++) acc0 = mfma32(G[s], y[s - s0], acc0);
    SCHED();
  }
  asm volatile("s_waitcnt vmcnt(0)" : "+v"(pc[5][0]), "+v"(pc[5][1]) :: "memory");
  acc0 = mfma32(sel[0], asbf8(pc[5][0]), acc0); acc0 = mfma32(sel[1], asbf8(pc[5][1]), acc0);
  char* op = p.out + (size_t)(32 * row + g) * 896 + j * 128 + T * 64 + h * 16;
#pragma unroll
  for (int t = 0; t < 2; t++) {
    u4 v = pack8(acc0, 8 * t);
    u2 a = __builtin_amdgcn_permlane32_swap(v[0], v[2], false, false), b = __builtin_amdgcn_permlane32_swap(v[1], v[3], false, false);
    *(u4*)(op + t * 32) = u4{a[0], b[0], a[1], b[1]};
  }
  if (blockIdx.x == 0 && wv == 0) flag(rws, 0, tag, l);
}

DI float red8(float v) {
  v += __builtin_bit_cast(float, __builtin_amdgcn_update_dpp(0, __builtin_bit_cast(int, v), 0xB1, 0xf, 0xf, true));
  v += __builtin_bit_cast(float, __builtin_amdgcn_update_dpp(0, __builtin_bit_cast(int, v), 0x4E, 0xf, 0xf, true));
  v += __builtin_bit_cast(float, __builtin_amdgcn_update_dpp(0, __builtin_bit_cast(int, v), 0x141, 0xf, 0xf, true));
  return v;
}
DI void a_wave(const Params& p, int g, int j, int ordb, int wv, int l) {
  const int a = wv - 4;
  const rsrc_t rx = mkrs(p.x, 2048 * 896), r1 = mkrs(p.w1, 448 * 3584), r2 = mkrs(p.w2, 1792 * 896), rws = mkrs(p.ws, WS_END);
  unsigned xo[2];
#pragma unroll
  for (int e = 0; e < 2; e++) { const int row = 16 * a + 8 * e + (l >> 3); xo[e] = (32 * row + g) * 896 + (((l & 7) ^ ((row >> 1) & 7)) << 4); }
  int ch[7];
#pragma unroll
  for (int i = 0; i < 7; i++) ch[i] = ordb ? (i + 3) % 7 : i;
  auto dmax = [&](int i) { dma16(rx, xo[0], ch[i] * 128, ch[i] * 8192 + a * 2048); dma16(rx, xo[1], ch[i] * 128, ch[i] * 8192 + a * 2048 + 1024); };
#pragma unroll
  for (int i = 0; i < 5; i++) dmax(i);
  const u64 ep = epoch_load(p.ws);
  SCHED();
  const unsigned swz = ((l & 7) ^ (((l >> 4) & 1) << 2)) << 4;
  const unsigned o1 = (8 * a + (l >> 3)) * 3584 + j * 512 + swz, o2 = (256 * j + 8 * a + (l >> 3)) * 896 + swz;
  const unsigned ring = L_RING + a * 1024;
  auto dmaw = [&](int v, int n) {
    if (v < 8) dma16(r1, o1, (v >> 1) * 128 + (224 * (v & 1) + 32 * n) * 3584, ring + (v % 3) * 32768 + n * 4096);
    else { int dst = j + v - 7; dst = dst >= 7 ? dst - 7 : dst; dma16(r2, o2, dst * 128 + n * 32 * 896, ring + (v % 3) * 32768 + n * 4096); }
  };
  const int k = 32 * j + g;
  const bool special = k == 0;
  Fft ft;
  ft.consts(a, l, special);
  ft.pin();
  SCHED();
  dmax(5); dmax(6);
  const u2 tag = epoch_tag(ep);
  f2 xv[7][2][4];
  f2 sm[2] = {{0.f, 0.f}, {0.f, 0.f}}, sq[2] = {{0.f, 0.f}, {0.f, 0.f}};
  const unsigned xl = a * 2048 + l * 16;
#pragma unroll
  for (int i = 0; i < 7; i++) {
    if (i == 0) WAITV(12); else if (i == 1) WAITV(10); else WAITV(8);
    BAR();
    u4 t[2];
#pragma unroll
    for (int e = 0; e < 2; e++) t[e] = asu4(lds128(xl + ch[i] * 8192 + e * 1024));
    if (i >= 2) { dmaw((2 * i - 4) / 7, (2 * i - 4) % 7); dmaw((2 * i - 3) / 7, (2 * i - 3) % 7); }
#pragma unroll
    for (int e = 0; e < 2; e++)
#pragma unroll
      for (int n = 0; n < 4; n++) {
        f2 v = {bflo(t[e][n]), bfhi(t[e][n])};
        xv[i][e][n] = v; sm[e] += v; sq[e] = __builtin_elementwise_fma(v, v, sq[e]);
      }
    SCHED();
  }
  f2 sc[2], of[2];
#pragma unroll
  for (int e = 0; e < 2; e++) {
    const float mean = red8(sm[e][0] + sm[e][1]) * (1.0f / 448.0f), ssq = red8(sq[e][0] + sq[e][1]) * (1.0f / 448.0f);
    const float var = __builtin_fmaxf(__builtin_fmaf(-mean, mean, ssq), 0.f);
    const float sd = rbf(__builtin_amdgcn_sqrtf(rbf(rbf(var) + 1e-5f)));
    const float r = __builtin_amdgcn_rcpf(sd), nm = -rbf(mean);
    *(LAS float*)(uintptr_t)(L_MT + (16 * a + 8 * e + (l >> 3)) * 4) = nm;
    *(LAS float*)(uintptr_t)(L_RT + (16 * a + 8 * e + (l >> 3)) * 4) = r;
    sc[e] = f2{r, r}; of[e] = f2{nm * r, nm * r};
  }
  WAITL0();
  BAR();
#pragma unroll
  for (int i = 0; i < 7; i++) {
#pragma unroll
    for (int e = 0; e < 2; e++) {
      u4 t;
#pragma unroll
      for (int n = 0; n < 4; n++) t[n] = pk2(__builtin_elementwise_fma(xv[i][e][n], sc[e], of[e]));
      *(LAS u4*)(uintptr_t)(xl + ch[i] * 8192 + e * 1024) = t;
      const int n = 10 + 2 * i + e - 3;
      if (n >= 10) dmaw(n / 7, n % 7);
      SCHED();
    }
  }
  WAITVL(14);
  BAR();
  WAITV(7);
  BAR();
  f4 av[4];
  u2 fl;
  const unsigned xfo = l < 32 ? WS_XF + j * 256 + l * 8 : WS_WF + (l - 32) * 8;
  const unsigned hw = L_H + (a & 1) * 16384 + (a >> 1) * 2048 + l * 16;
#pragma unroll
  for (int v = 1; v < 14; v++) {
    const int nd = v + 2 < 8 ? 7 : (v + 2 < 15 ? 8 : 0);
    if (v == POLL_ROUND) fl = ld8s(rws, xfo);
    if (v == POLL_ROUND + 1) ft.loads(rws, k, a, l);
    if (v <= 8 && !(v & 1)) {
#pragma unroll
      for (int e = 0; e < 4; e++) av[e] = ldsf4(L_ACC + a * 4096 + e * 1024 + l * 16);
    }
#pragma unroll
    for (int n = 0; n < 3; n++) if (n < nd) dmaw(v + 2, n);
    SCHED();
    if (v >= 2 && v <= 8) *(LAS u4*)(uintptr_t)(hw + ((v - 2) >> 1) * 4096 + (v & 1) * 1024) = gelu8(av[2 * (v & 1)], av[2 * (v & 1) + 1]);
    if (v == 9) ft.stageA(special);
    if (v == 10) ft.stageD(rws, k, a, l, special);
    SCHED();
#pragma unroll
    for (int n = 3; n < 5; n++) if (n < nd) dmaw(v + 2, n);
    SCHED();
    if (v == 8) {
      *(LAS u4*)(uintptr_t)(hw + 3 * 4096 + 1024) = gelu8(av[2], av[3]);
      WAITL0();
      BAR();
    }
    if (v == 9) { ft.stageB(); SCHED(); dmaw(v + 2, 5); SCHED(); ft.stageC(a, l); SCHED(); }
#pragma unroll
    for (int n = (v == 9 ? 6 : 5); n < 8; n++) if (n < nd) dmaw(v + 2, n);
    if (v == 10) WAITVL(10); else WAITVL(nd);
    if (v == POLL_ROUND) { TIE(fl); if (!match(fl, tag)) poll(rws, xfo, tag); }
    if (v == POLL_ROUND + 1) ft.arrived();
    if (v == 11) arrive_flag(rws, yflag_off(k), tag, l);
    BAR();
  }
  poll(rws, WS_YF + a * 512 + (l < 56 || (l == 56 && a == 0) ? l * 8 : 0), tag);
  u4 y[14];
  const unsigned yo = WS_Y1 + (4 * a + (l >> 4)) * 8192 + g * 256 + (l & 15) * 16;
#pragma unroll
  for (int n = 0; n < 14; n++) y[n] = ld16s(rws, yo + n * 131072, 0);
  const unsigned yw = L_Y + (4 * a + (l >> 4)) * 256 + (((l & 15) ^ (((l >> 4) & 1) << 3)) << 4);
#pragma unroll
  for (int n = 0; n < 14; n++) {
    asm volatile("s_waitcnt vmcnt(%1)" : "+v"(y[n]) : "i"(13 - n) : "memory");
    *(LAS u4*)(uintptr_t)(yw + n * 4096) = y[n];
    if (n == 3 || n == 7 || n == 10 || n == 13) { WAITL0(); BAR(); }
  }
}

DI void helper(const Params& p, int id, int wv, int l, u64 pc) {
  const int tid = threadIdx.x;
  const rsrc_t rws = mkrs(p.ws, WS_END);
  unsigned d0, d1, d2;
  {
    const rsrc_t rc = mkrs((const void*)pc, CODE_LINES * 128), r1 = mkrs(p.w1, 448 * 3584);
    asm volatile("buffer_load_dword %0, %1, %2, 0 offen" : "=v"(d0) : "v"(tid * 128), "s"(rc) : "memory");
    const int e = (id * 2 + (wv & 1)) * 49 + l;
    asm volatile("buffer_load_dword %0, %1, %2, 0 offen" : "=v"(d1) : "v"(l < 49 ? e * 512 + (wv >> 1) * 128 : 0x7fffffff), "s"(r1) : "memory");
  }
  const int w4 = id >> 3, f20 = (id & 7) * 4;
  {
    const rsrc_t rw = mkrs(wv >= 4 ? p.wi : p.wr, 2048 * 450);
    const unsigned so = (16 * w4 + 64 * (f20 + (wv & 3))) * 450;
#pragma unroll
    for (int n = 0; n < 8; n++) {
      const unsigned vo = (n < 7 || l < 2) ? n * 1024 + l * 16 : 0x7fffffffu;
      asm volatile("s_mov_b32 m0, %3\n\ts_nop 0\n\tbuffer_load_dwordx4 %0, %1, %2 offen nt lds" :: "v"(vo), "s"(rw), "s"(so), "s"(16384 + wv * 8192 + n * 1024) : "memory");
    }
    const rsrc_t r2 = mkrs(p.w2, 1792 * 896);
    asm volatile("buffer_load_dword %0, %1, %2, 0 offen" : "=v"(d2) : "v"(l < 49 ? ((id * 8 + wv) * 49 + l) * 128 : 0x7fffffff), "s"(r2) : "memory");
  }
  if (tid == 0) *(LAS unsigned*)(uintptr_t)L_CNT = 0u;
  const u2 tag = epoch_tag(epoch_load(p.ws));
  WAITVL(1);
  BAR();
  for (int e = tid; e < 3600; e += 512) {
    const int k = e >> 4, c = e & 15;
    u4 v;
#pragma unroll
    for (int i = 0; i < 4; i++) {
      unsigned a = *(const LAS unsigned short*)(uintptr_t)(16384 + i * 8192 + c * 450 + k * 2);
      unsigned b = *(const LAS unsigned short*)(uintptr_t)(16384 + 32768 + i * 8192 + c * 450 + k * 2);
      v[i] = a | (b << 16);
    }
    st16s(v, rws, WS_WT + k * 8192 + id * 256 + c * 16, 0);
  }
  asm volatile("s_waitcnt vmcnt(0)" : "+v"(d0), "+v"(d1), "+v"(d2) :: "memory");
  BAR();
  if (wv == 0) flag(rws, WS_WF + id * 8, tag, l);
  if (id != 0 || wv >= 4) return;
  Fft ft;
  ft.consts(wv, l, true);
  ft.pin();
  poll(rws, l < 32 ? WS_XF + l * 8 : WS_WF + (l - 32) * 8, tag);
  ft.loads(rws, 224, wv, l);
  WAITV(0);
  ft.arrived();
  ft.stageA(true); ft.stageB(); ft.stageC(wv, l);
  WAITL0();
  BAR();
  ft.stageD(rws, 224, wv, l, true);
  WAITV(0);
  arrive_flag(rws, yflag_off(224), tag, l);
}

__global__ __launch_bounds__(512, 2) void k_fused(Params p) {
  u64 pc; asm volatile("s_getpc_b64 %0" : "=s"(pc));
  const int b = blockIdx.x, xc = b & 7, i = b >> 3;
  const int l = threadIdx.x & 63, wv = __builtin_amdgcn_readfirstlane(threadIdx.x >> 6);
  if (i >= 28) { helper(p, xc * 4 + (i - 28), wv, l, pc & ~127ull); return; }
  const int j = i < 24 ? (xc & 1) * 4 + i % 3 : 3;
  const int g = (xc >> 1) * 8 + (i < 24 ? i / 3 : (xc & 1) * 4 + i - 24);
  const int ordb = j & 1;
  if (wv < 4) g_wave(p, g, j, ordb, wv, l); else a_wave(p, g, j, ordb, wv, l);
}

extern "C" void kernel_launch(void* const* d_in, const int* in_sizes, int n_in, void* d_out, int out_size, void* d_ws, size_t ws_size, hipStream_t stream) {
  Params p;
  p.x = (const char*)d_in[0]; p.wr = (const char*)d_in[1]; p.wi = (const char*)d_in[2]; p.w1 = (const char*)d_in[4];
  p.b1 = (const char*)d_in[5]; p.w2 = (const char*)d_in[6]; p.b2 = (const char*)d_in[7]; p.out = (char*)d_out; p.ws = (char*)d_ws;
  (void)hipFuncSetAttribute((const void*)k_fused, hipFuncAttributeMaxDynamicSharedMemorySize, L_TOTAL);
  k_fused<<<256, 512, L_TOTAL, stream>>>(p);
}
```

```cpp
#define CODE_LINES 262
#include <hip/hip_runtime.h>
#include <stdint.h>

typedef __bf16 bf2 __attribute__((ext_vector_type(2)));
typedef __bf16 bf4 __attribute__((ext_vector_type(4)));
typedef __bf16 bf8 __attribute__((ext_vector_type(8)));
typedef __bf16 bf16v __attribute__((ext_vector_type(16)));
typedef float f2 __attribute__((ext_vector_type(2)));
typedef float f4 __attribute__((ext_vector_type(4)));
typedef float f16v __attribute__((ext_vector_type(16)));
typedef unsigned u2 __attribute__((ext_vector_type(2)));
typedef unsigned u4 __attribute__((ext_vector_type(4)));
typedef unsigned long long u64;
#define LAS __attribute__((address_space(3)))
#define DI __device__ __forceinline__
typedef __amdgpu_buffer_rsrc_t rsrc_t;

#define AI __attribute__((always_inline))
template <int I> struct IC { static constexpr int v = I; };
template <int B, int E, class F> DI void sfor(F&& f) { if constexpr (B < E) { f(IC<B>{}); sfor<B + 1, E>(f); } }

constexpr int WS_XF = 4096;
constexpr int WS_WF = WS_XF + 7168;
constexpr int WS_PF = 16384;
#define CSW 8448
#define CSX 8448
#define CSY 8448
#define GSY 256
#define PSZ 2048
constexpr int WS_WT = 524288;
constexpr int WS_X1 = WS_WT + 256 * 8704;
constexpr int WS_Y1 = WS_X1 + 256 * 8704;
constexpr int WS_P = WS_Y1 + 256 * 8704;
constexpr int WS_END = WS_P + 32 * 7 * 24 * PSZ;
constexpr unsigned L_Z = 0, L_FX = 8192, L_H = 16384, L_WT = 49152, L_RING = 57344, L_Y = L_RING, L_EX = 0;
constexpr unsigned L_MT = 155648, L_DUM = L_MT + 1024, L_SY = L_DUM + 512;
constexpr unsigned L_TOTAL = 163840;
#define EXP 0
constexpr int SPIN_CAP = (EXP & 1) ? 2 : 20000;
#define PRE 10
#define PS 0
#define BA0 10
#define BA1 18
#define GB0 2
#define GB1 7
#define G2A 3
#define G2B 9
#define G2C 18
#define NB 3
#define LATE 0
#define XFS 10
#define XPOLL 92
#define XCHK 104
#define FL0 74
#define FL1 100
#define PPOLL 200
#define TF 0
#define TP 99
#define TM 8
#define OAUX 18
#define OPAT 1
#define X0 " sc1"
#define X1_ " sc1 nt"
#define X2 " sc0 sc1"
#define X3 " sc0 sc1 nt"
#define AL X0
#define AF X0
#define AS X0
#define AG X0
#define YROT 0
#define PONE 0
#define XL 1
#define FG 99
#define XB 6
#define XD 6
#define FS 4
#define SB 19
#define XST -1
#define XSP 2
#define ROT 6
#define XPRIO 2
#define EXS 1
#define FB 2
#define SPRIO 2
#define TW0 8
#define IPRIO 0
#define F2PRIO 2
#define F3PRIO 2
#define YS 9
#define YS2 13
#define HF 0
#define SWAP 0
#define KARG 0
#define AX ""
#define AW ""
#define FV 0
#define FI 0
#define STE 4
#define PP0 200
#define PC0 200
#define PB 1
#define PBS 6
#define TG 1
#define T1 0
#define T2 14
#define PA 1
#define HD 0
#define GPRIO 0
#define MPRIO 1
#define FPRIO 2
#define GG 11
#define YSL 2
#define TWF 3
#define EPRIO 2
#define M2PRIO 1
#define TAG2 0
#define EARLY 0
#define PDEL 0
#define PSL 1
#define SP0 2
#define SP1 2
#define SP2 2
#define YG 56
#define RATE 2
#define XA 3
#define OWN 72
#define BM0 4
#define BM1 12
#define IB 0
#define GF 14
constexpr float GS = -0.46866869f;
constexpr float GK = 4.91222954f;

struct Params {
  const char* x; const char* wr; const char* wi; const char* w1; const char* b1; const char* w2; const char* b2;
  char* out; char* ws;
};

DI unsigned pk(float a, float b) { f2 v = {a, b}; bf2 r = __builtin_convertvector(v, bf2); return __builtin_bit_cast(unsigned, r); }
DI unsigned pk2(f2 v) { bf2 r = __builtin_convertvector(v, bf2); return __builtin_bit_cast(unsigned, r); }
DI float rbf(float a) { return __uint_as_float(pk(a, a) << 16); }
DI float bflo(unsigned u) { return __uint_as_float(u << 16); }
DI float bfhi(unsigned u) { return __uint_as_float(u & 0xffff0000u); }
DI bf8 asbf8(u4 v) { return __builtin_bit_cast(bf8, v); }
DI u4 asu4(bf8 v) { return __builtin_bit_cast(u4, v); }
DI f16v mfma32(bf8 a, bf8 b, f16v c) { return __builtin_amdgcn_mfma_f32_32x32x16_bf16(a, b, c, 0, 0, 0); }
DI f4 mfma16(bf8 a, bf8 b, f4 c) { return __builtin_amdgcn_mfma_f32_16x16x32_bf16(a, b, c, 0, 0, 0); }
DI bf8 lds128(unsigned a) { return *(const LAS bf8*)(uintptr_t)a; }
DI f4 ldsf4(unsigned a) { return *(const LAS f4*)(uintptr_t)a; }
DI bf4 ldstr(unsigned a) { return __builtin_amdgcn_ds_read_tr16_b64_v4bf16((LAS bf4*)(uintptr_t)a); }
DI bf8 cat(bf4 a, bf4 b) { return __builtin_shufflevector(a, b, 0, 1, 2, 3, 4, 5, 6, 7); }
DI float cosr(float rev) { return __builtin_amdgcn_cosf(rev); }
DI float sinr(float rev) { return __builtin_amdgcn_sinf(rev); }
DI rsrc_t mkrs(const void* p, int n) { return __builtin_amdgcn_make_buffer_rsrc((void*)p, 0, n, 0x00020000); }
DI void setm0(unsigned base, int off) { asm volatile("s_add_u32 m0, %0, %1" :: "s"(base), "i"(off) : "memory", "scc"); }
DI void setm0n(unsigned base, int off) { asm volatile("s_add_u32 m0, %0, %1\n\ts_nop 0" :: "s"(base), "i"(off) : "memory", "scc"); }
DI void dma16(rsrc_t rs, unsigned voff, unsigned soff, int imm) {
  asm volatile("buffer_load_dwordx4 %0, %1, %2 offen offset:%3" AW " lds" :: "v"(voff), "s"(rs), "s"(soff), "i"(imm) : "memory");
}
DI void dma4(rsrc_t rs, unsigned voff, unsigned soff) { asm volatile("buffer_load_dword %0, %1, %2 offen lds" :: "v"(voff), "s"(rs), "s"(soff) : "memory"); }
DI void dma4s(rsrc_t rs, unsigned voff, unsigned soff, int imm) { asm volatile("buffer_load_dword %0, %1, %2 offen offset:%3" AF " lds" :: "v"(voff), "s"(rs), "s"(soff), "i"(imm) : "memory"); }
DI void dma16s(rsrc_t rs, unsigned voff, unsigned soff) {
  asm volatile("buffer_load_dwordx4 %0, %1, %2 offen" AL " lds" :: "v"(voff), "s"(rs), "s"(soff) : "memory");
}
DI u4 ld16s(rsrc_t rs, unsigned voff, int imm) {
  u4 t; asm volatile("buffer_load_dwordx4 %0, %1, %2, 0 offen offset:%3" AL "" : "=v"(t) : "v"(voff), "s"(rs), "i"(imm) : "memory"); return t;
}
DI u2 ld8s(rsrc_t rs, unsigned voff) {
  u2 t; asm volatile("buffer_load_dwordx2 %0, %1, %2, 0 offen" AF "" : "=v"(t) : "v"(voff), "s"(rs) : "memory"); return t;
}
DI void st16s(u4 v, rsrc_t rs, unsigned voff, int imm) { asm volatile("buffer_store_dwordx4 %0, %1, %2, 0 offen offset:%3" AS "\n\ts_nop 1" :: "v"(v), "v"(voff), "s"(rs), "i"(imm) : "memory"); }
DI void st8s(u2 v, rsrc_t rs, unsigned voff) { asm volatile("buffer_store_dwordx2 %0, %1, %2, 0 offen" AG "" :: "v"(v), "v"(voff), "s"(rs) : "memory"); }
#define WAITV(n) asm volatile("s_waitcnt vmcnt(%0)" :: "i"(n) : "memory")
#define WAITVL(n) asm volatile("s_waitcnt vmcnt(%0) lgkmcnt(0)" :: "i"(n) : "memory")
#define WAITL0() asm volatile("s_waitcnt lgkmcnt(0)" ::: "memory")
#define TIE(x) asm volatile("" : "+v"(x))
#define TIES(x) asm volatile("" : "+s"(x))
#define WAITLK(n) __builtin_amdgcn_s_waitcnt(0xC07F | ((n) << 8))
#define BAR() do { asm volatile("" ::: "memory"); __builtin_amdgcn_s_barrier(); asm volatile("" ::: "memory"); } while (0)
#define SCHED() __builtin_amdgcn_sched_barrier(0)

DI bool match(u2 v, u2 tag) { return __builtin_amdgcn_ballot_w64(v[0] != tag[0] || v[1] != tag[1]) == 0; }
DI void poll(rsrc_t rs, unsigned voff, u2 tag) {
#pragma unroll 1
  for (int n = 0; n < SPIN_CAP; n++) {
    u2 v = ld8s(rs, voff);
    asm volatile("s_waitcnt vmcnt(0)" : "+v"(v) :: "memory");
    if (match(v, tag)) break;
    __builtin_amdgcn_s_sleep(PSL);
  }
}
DI u64 epoch_load(const char* ws) { u64 e; asm volatile("s_load_dwordx2 %0, %1, 0x0" : "=s"(e) : "s"(ws) : "memory"); return e; }
DI u2 epoch_tag(u64 e) {
  asm volatile("s_waitcnt lgkmcnt(0)" : "+s"(e) :: "memory");
  e = e * 6364136223846793005ull + 1442695040888963407ull;
  return u2{(unsigned)e, (unsigned)(e >> 32)};
}
DI void flag(rsrc_t rs, unsigned off, u2 tag, int l) {
  asm volatile("buffer_store_dwordx2 %0, %1, %2, %3 offen" AG "" :: "v"(tag), "v"(l ? 0x7ffffff0u : 0u), "s"(rs), "s"(off) : "memory");
}

DI void gen14(bf8* out, f16v& sum, float a0, float th, float dn) {
  float u[2][8];
#pragma unroll
  for (int i = 0; i < 8; i++) { const float e = __builtin_fmaf((float)i, th, a0); u[0][i] = cosr(e); u[1][i] = cosr(e + dn); }
  const float c2 = 2.f * cosr(dn);
  const bf8 ones = asbf8(u4{0x3f803f80u, 0x3f803f80u, 0x3f803f80u, 0x3f803f80u});
#pragma unroll
  for (int n = 0; n < 14; n++) {
    float* v = u[n & 1];
    out[n] = asbf8(u4{pk(v[0], v[1]), pk(v[2], v[3]), pk(v[4], v[5]), pk(v[6], v[7])});
    SCHED();
    if (n < 12) {
#pragma unroll
      for (int i = 0; i < 8; i += 2) {
        f2 t = __builtin_elementwise_fma(f2{c2, c2}, f2{u[(n & 1) ^ 1][i], u[(n & 1) ^ 1][i + 1]}, -f2{v[i], v[i + 1]});
        v[i] = t[0]; v[i + 1] = t[1];
      }
    }
    SCHED();
    sum = mfma32(ones, out[n], sum);
    TIE(sum);
    SCHED();
  }
}
DI u4 pack8(const f16v& a, int o) { return u4{pk(a[o], a[o + 1]), pk(a[o + 2], a[o + 3]), pk(a[o + 4], a[o + 5]), pk(a[o + 6], a[o + 7])}; }

DI bf8 gen8(float a0, float da_lo, float da_hi) {
  float v[8];
#pragma unroll
  for (int j = 0; j < 8; j++) v[j] = cosr(a0 + (float)(j & 3) * da_lo + (float)(j >> 2) * da_hi);
  return asbf8(u4{pk(v[0], v[1]), pk(v[2], v[3]), pk(v[4], v[5]), pk(v[6], v[7])});
}
DI bf8 neg8(bf8 a) { u4 v = asu4(a); return asbf8(v ^ 0x80008000u); }
DI bf8 pack44(f4 a, f4 b) { return asbf8(u4{pk(a[0], a[1]), pk(a[2], a[3]), pk(b[0], b[1]), pk(b[2], b[3])}); }
DI bf8 xor8(bf8 a, unsigned m) { u4 v = asu4(a); return asbf8(v ^ m); }
struct Fft {
  bf8 C64[2], S64[2], N64[2], C32[2], S32[2], N32[2];
  u2 cb, sb;
  float tc[2][4], ts[2][4], ic[2][4], is[2][4];
  u4 wt[2];
  bf8 pr, pi;
  DI void consts(int w4, int l) {
    const int c = l & 15, q = l >> 4;
    const float f1 = (float)(16 * w4 + c);
    const float d = f1 * (1.0f / 64.0f), a0 = d * (float)(16 * (q >> 1) + 4 * (q & 1));
    C64[0] = gen8(a0, d, 8.f * d); S64[0] = gen8(a0 - 0.25f, d, 8.f * d);
    const float d2 = (float)c * (1.0f / 32.0f), b0 = d2 * (float)(4 * q);
    cb = u2{pk(cosr(b0), cosr(b0 + d2)), pk(cosr(b0 + 2.f * d2), cosr(b0 + 3.f * d2))};
    sb = u2{pk(sinr(b0), sinr(b0 + d2)), pk(sinr(b0 + 2.f * d2), sinr(b0 + 3.f * d2))};
    TIE(C64[0]); TIE(S64[0]); TIE(cb); TIE(sb);
  }
  template <int u> DI void twiddles(int w4, int l) {
    const int c = l & 15, q = l >> 4;
    const float e = (float)(16 * w4 + c) * (1.0f / 2048.0f), e0 = e * (float)(4 * q), r0 = (float)(16 * w4 + 4 * q);
    {
      const float g = (float)(16 * u + c) * (1.0f / 2048.0f), g0 = g * r0;
#pragma unroll
      for (int i = 0; i < 4; i++) {
        const float a = __builtin_fmaf((float)(16 * u + i), e, e0), b = __builtin_fmaf((float)i, g, g0);
        tc[u][i] = cosr(a); ts[u][i] = sinr(a); ic[u][i] = cosr(b); is[u][i] = sinr(b);
        TIE(tc[u][i]); TIE(ts[u][i]); TIE(ic[u][i]); TIE(is[u][i]);
      }
    }
  }
  float te, te0, tg[2], tr;
  DI void tw0(int w4, int l) {
    const int c = l & 15, q = l >> 4;
    te = (float)(16 * w4 + c) * (1.0f / 2048.0f); te0 = te * (float)(4 * q); tr = (float)(16 * w4 + 4 * q);
    tg[0] = (float)c * (1.0f / 2048.0f); tg[1] = tg[0] + 16.0f / 2048.0f;
    TIE(te); TIE(te0); TIE(tr); TIE(tg[0]); TIE(tg[1]);
  }
  template <int n> DI void tw1() {
    constexpr int u = (n >> 2) & 1, i = n & 3;
    if constexpr (n < 8) { const float a = __builtin_fmaf((float)(16 * u + i), te, te0); tc[u][i] = cosr(a); ts[u][i] = sinr(a); TIE(tc[u][i]); TIE(ts[u][i]); }
    else { const float b = (tr + (float)i) * tg[u]; ic[u][i] = cosr(b); is[u][i] = sinr(b); TIE(ic[u][i]); TIE(is[u][i]); }
  }
  template <int I> DI void expand(unsigned sm) {
    if constexpr (I == 0) C64[1] = xor8(C64[0], sm);
    if constexpr (I == 1) S64[1] = xor8(S64[0], sm);
    if constexpr (I == 2) N64[0] = neg8(S64[0]);
    if constexpr (I == 3) N64[1] = neg8(S64[1]);
    if constexpr (I == 4) { C32[0] = asbf8(u4{cb[0], cb[1], cb[0] ^ sm, cb[1] ^ sm}); S32[0] = asbf8(u4{sb[0], sb[1], sb[0] ^ sm, sb[1] ^ sm}); TIE(S32[0]); }
    if constexpr (I == 5) C32[1] = xor8(C32[0], 0x80000000u);
    if constexpr (I == 6) S32[1] = xor8(S32[0], 0x80000000u);
    if constexpr (I == 7) N32[0] = neg8(S32[0]);
    if constexpr (I == 8) N32[1] = xor8(S32[0], 0x00008000u);
    bf8& r = I == 0 ? C64[1] : I == 1 ? S64[1] : I == 2 ? N64[0] : I == 3 ? N64[1] : I == 4 ? C32[0] : I == 5 ? C32[1] : I == 6 ? S32[1] : I == 7 ? N32[0] : N32[1];
    TIE(r);
  }
  unsigned zb, wo, yo;
  DI void addr(int k, int w4, int l) {
    const int c = l & 15, q = l >> 4;
    zb = L_Z + c * 256 + (((q + (k == 224 ? 4 : 0)) ^ c) << 4);
    wo = L_FX + (w4 >> 1) * 1024 + ((2 * (w4 & 1) + (q & 1)) * 16 + c) * 16 + (q >> 1) * 8;
    yo = WS_Y1 + (k == 224 ? 8 : k * CSY) + c * GSY + w4 * 64 + q * 16;
    if (EXP & 32) yo = 0x7ff00000u;
    TIE(zb); TIE(wo); TIE(yo);
  }
  static DI void cmul(f4& re, f4& im, f4 a, f4 b, const float* c, const float* s, bool conj) {
#pragma unroll
    for (int i = 0; i < 4; i++) { re[i] = a[i] * c[i]; SCHED(); im[i] = b[i] * c[i]; SCHED(); }
#pragma unroll
    for (int i = 0; i < 4; i++) {
      float x = __builtin_fmaf(conj ? b[i] : -b[i], s[i], re[i]); TIE(x); re[i] = x; SCHED();
      float y = __builtin_fmaf(conj ? -a[i] : a[i], s[i], im[i]); TIE(y); im[i] = y; SCHED();
    }
  }
  DI void stageABC(int w4, unsigned l16, bool special) {
    const f4 zero = {0.f, 0.f, 0.f, 0.f};
    u4 z[2][2][2];
#pragma unroll
    for (int u = 0; u < 2; u++)
#pragma unroll
      for (int e = 0; e < 4; e++) z[u][e >> 1][e & 1] = asu4(lds128((zb ^ ((e >> 1) * 64 + (e & 1) * 128)) + u * 4096));
    wt[0] = asu4(lds128(L_WT + w4 * 2048 + l16)); wt[1] = asu4(lds128(L_WT + 1024 + w4 * 2048 + l16));
    SCHED();
    f4 re[2], im[2], a[2], b[2];
    if (special) { z[0][1][0] = z[0][1][1] = z[1][1][0] = z[1][1][1] = u4{0u, 0u, 0u, 0u}; }
    if (FI) {
#pragma unroll
      for (int e = 0; e < 4; e++)
#pragma unroll
        for (int u = 0; u < 2; u++) {
          const bf8 v = asbf8(z[u][e >> 1][e & 1]);
          a[u] = mfma16(v, e < 2 ? C64[e & 1] : S64[e & 1], e ? a[u] : zero); TIE(a[u]);
          b[u] = mfma16(v, e < 2 ? N64[e & 1] : C64[e & 1], e ? b[u] : zero); TIE(b[u]);
          SCHED();
        }
    } else
#pragma unroll
    for (int u = 0; u < 2; u++) {
      bf8 r0 = asbf8(z[u][0][0]), r1 = asbf8(z[u][0][1]), i0 = asbf8(z[u][1][0]), i1 = asbf8(z[u][1][1]);
      a[u] = mfma16(r0, C64[0], zero); b[u] = mfma16(r0, N64[0], zero);
      a[u] = mfma16(r1, C64[1], a[u]); b[u] = mfma16(r1, N64[1], b[u]);
      a[u] = mfma16(i0, S64[0], a[u]); b[u] = mfma16(i0, C64[0], b[u]);
      a[u] = mfma16(i1, S64[1], a[u]); b[u] = mfma16(i1, C64[1], b[u]);
    }
    SCHED();
    __builtin_amdgcn_s_setprio(FV);
    float wr[2][4], wi[2][4];
    cmul(re[0], im[0], a[0], b[0], tc[0], ts[0], true);
    cmul(re[1], im[1], a[1], b[1], tc[1], ts[1], true);
    pr = pack44(re[0], re[1]); pi = pack44(im[0], im[1]);
    if (FV != F2PRIO) { TIE(pr); TIE(pi); SCHED(); __builtin_amdgcn_s_setprio(F2PRIO); }
#pragma unroll
    for (int u = 0; u < 2; u++) {
      a[u] = mfma16(C32[u], pr, zero); b[u] = mfma16(N32[u], pr, zero);
      a[u] = mfma16(S32[u], pi, a[u]); b[u] = mfma16(C32[u], pi, b[u]);
    }
#pragma unroll
    for (int e = 0; e < 8; e++) { wr[e >> 2][e & 3] = bflo(wt[e >> 2][e & 3]); wi[e >> 2][e & 3] = bfhi(wt[e >> 2][e & 3]); }
    SCHED();
    __builtin_amdgcn_s_setprio(FV);
    cmul(re[0], im[0], a[0], b[0], wr[0], wi[0], false);
    cmul(re[1], im[1], a[1], b[1], wr[1], wi[1], false);
    pr = pack44(re[0], re[1]); pi = pack44(im[0], im[1]);
    if (FV != F2PRIO) { TIE(pr); TIE(pi); SCHED(); __builtin_amdgcn_s_setprio(F2PRIO); }
#pragma unroll
    for (int u = 0; u < 2; u++) {
      a[u] = mfma16(pr, C32[u], zero); b[u] = mfma16(pr, S32[u], zero);
      a[u] = mfma16(pi, N32[u], a[u]); b[u] = mfma16(pi, C32[u], b[u]);
    }
    SCHED();
    __builtin_amdgcn_s_setprio(FV);
#pragma unroll
    for (int u = 0; u < 2; u++) {
      cmul(re[u], im[u], a[u], b[u], ic[u], is[u], false);
      *(LAS u2*)(uintptr_t)(wo + (u * 2 + 0) * 2048) = u2{pk(re[u][0], re[u][1]), pk(re[u][2], re[u][3])};
      *(LAS u2*)(uintptr_t)(wo + (u * 2 + 1) * 2048) = u2{pk(im[u][0], im[u][1]), pk(im[u][2], im[u][3])};
      SCHED();
    }
  }
  bf8 ux[2][4];
  DI void readD(unsigned l16) {
#pragma unroll
    for (int e = 0; e < 8; e++) ux[e >> 2][e & 3] = lds128(L_FX + e * 1024 + l16);
  }
  f4 da[2], db[2];
  DI void mulD() {
    const f4 zero = {0.f, 0.f, 0.f, 0.f};
    if (FI) {
#pragma unroll
      for (int e = 0; e < 4; e++)
#pragma unroll
        for (int u = 0; u < 2; u++) {
          da[u] = mfma16(e < 2 ? C64[e & 1] : N64[e & 1], ux[u][e], e ? da[u] : zero); TIE(da[u]);
          db[u] = mfma16(e < 2 ? S64[e & 1] : C64[e & 1], ux[u][e], e ? db[u] : zero); TIE(db[u]);
          SCHED();
        }
    } else
#pragma unroll
    for (int u = 0; u < 2; u++) {
      f4 a = mfma16(C64[0], ux[u][0], zero), b = mfma16(S64[0], ux[u][0], zero);
      a = mfma16(C64[1], ux[u][1], a); b = mfma16(S64[1], ux[u][1], b);
      a = mfma16(N64[0], ux[u][2], a); b = mfma16(C64[0], ux[u][2], b);
      a = mfma16(N64[1], ux[u][3], a); b = mfma16(C64[1], ux[u][3], b);
      TIE(a); TIE(b);
      da[u] = a; db[u] = b;
    }
  }
  DI void storeD(rsrc_t rws, bool special) {
    u4 v = asu4(pack44(da[0], db[0])), w = asu4(pack44(da[1], db[1]));
    v[0] = min(v[0], ~1u); v[2] = min(v[2], ~1u); w[0] = min(w[0], ~1u); w[2] = min(w[2], ~1u);
    if (!special) { st16s(v, rws, yo, 0); st16s(w, rws, yo + 16 * GSY, 0); }
    else { st8s(u2{v[0], v[1]}, rws, yo); st8s(u2{w[0], w[1]}, rws, yo + 16 * GSY); }
  }
};
DI void fft_loads(rsrc_t rws, int k, int w4, int l) {
  const int kx = k == 224 ? 0 : k;
#pragma unroll
  for (int e = 0; e < 2; e++) {
    const int row = 8 * w4 + 4 * e + (l >> 4);
    setm0n(w4 * 2048, L_Z + e * 1024);
    dma16s(rws, row * 256 + (((l ^ row) & 15) << 4), WS_X1 + kx * CSX);
  }
#pragma unroll
  for (int e = 0; e < 2; e++) { setm0n(w4 * 2048, L_WT + e * 1024); dma16s(rws, l * 16, WS_WT + k * CSW + w4 * 2048 + e * 1024); }
}
DI unsigned xflag_off(int g, int j, int w4, int l) {
  unsigned a = WS_XF + j * 1024 + w4 * 256, b = WS_WF + w4 * 64, c = WS_XF + g * 32 + w4 * 8;
  TIES(a); TIES(b); TIES(c);
  return l < 32 ? a + l * 8 : l < 57 ? b + (l & 7) * 8 : c + (l - 57) * 1024;
}
DI bool match4(u4 a, u4 b, u2 t) {
  return __builtin_amdgcn_ballot_w64((((a[0] ^ t[0]) | (a[1] ^ t[1])) | ((a[2] ^ t[0]) | (a[3] ^ t[1])) | ((b[0] ^ t[0]) | (b[1] ^ t[1])) | ((b[2] ^ t[0]) | (b[3] ^ t[1]))) != 0) == 0;
}

constexpr bool isbar(int n) {
  if (n < 0 || n >= 112) return false;
  const int s = n % 28;
  return s == 24 || (n < 28 ? s == BA0 || s == BA1 : s == BM0 || s == BM1);
}
constexpr int grp_u(int i) { return i < 3 ? 0 : i < 3 + LATE ? 1 : i == 3 + LATE ? 0 : i >> 2; }
constexpr int grp_q(int i) { return i < 3 ? i : i < 3 + LATE ? i - 3 : i == 3 + LATE ? 3 : i < 8 ? i - 4 : i & 3; }
constexpr int cert(int n) { int m = n + 1; while (m < 112 && !isbar(m)) m++; return (m + 3) / 4 > 27 ? 27 : (m + 3) / 4; }
constexpr int room(int n) { while (n >= 0 && !isbar(n)) n--; return n < 0 ? 11 : n == 108 ? 39 : (n + 2) / 4 + 11; }
constexpr int XRES = 84, NG = 23;
constexpr int GP(int t) { return 224 + t; }
constexpr int DP(int s) { return 256 + s; }
struct Sched {
  int pc[288];
  int b0, b1;
  int st[3][8], fl[3];
  int w[2][113];
  int gw[24];
  int rd[48];
  int xw, pw, ow, fw[3], rw, qw, cw, lag, late;
};
constexpr Sched mksched() {
  Sched s{};
  for (int i = 0; i < 288; i++) s.pc[i] = -1;
  {
    int gaps[28] = {}, ng = 0;
    for (int i = PS; i < 28; i++) if ((FS ? !(FS == 2 && i == 11) && !(FS == 3 && i == 19) && !(FS == 5 && i == SB) : (i & 7) != 3 || i == 27) && i != 26) gaps[ng++] = i;
    for (int d = 0; d < 2 * (PRE - PA); d++) s.pc[DP(gaps[d * ng / (2 * (PRE - PA))])] = 2 * PA + d;
  }
  int q = PRE;
  auto pair = [&](int a, int b, int lim) { if (q <= lim && q < 56) { s.pc[a] = 2 * q; s.pc[b] = 2 * q + 1; q++; return true; } return false; };
  for (int n = 1; n < 84; n += 2) if (n % 28 != 27 && !pair(n, n + 1, room(n) - FG) && pair(n, n + 2, room(n))) n += 2;
  for (int t = 0; t + 1 < NG; t++) if (pair(GP(t), GP(t + 1), room(t > G2C ? 108 : t > G2B ? 84 + BM1 : t > G2A ? 84 + BM0 : 80))) t += 2;
  s.b0 = q; for (int i = 0; i < NB && q <= 39; i++) q++;
  s.b1 = q;
  int last = 0, is[56] = {};
  for (int r = 1; q < 56; r += RATE) { is[q] = r; pair(112 + r, 112 + r + RATE / 2, 55); last = r + RATE / 2; }
  for (int p = 47, nx = 1000; p >= 0; p--) {
    int d = 2 * p - 4;
    const int dl = p < 32 ? is[40 + p / 2] - 4 : p < 46 ? YG - 1 : d;
    if (dl < d) d = dl;
    if (nx - 1 < d) d = nx - 1;
    s.rd[p] = nx = d;
  }
  s.lag = 1000;
  for (int i = s.b1; i < 52; i++) if (s.rd[2 * i - 56] - is[i] < s.lag) s.lag = s.rd[2 * i - 56] - is[i];
  {
    int n = last + 1 - STE > 34 + 8 * LATE ? last + 1 - STE : 34 + 8 * LATE;
    for (int e = 0; e < 4; e++) s.st[0][e] = n + SP0 * e;
    n = 66;
    for (int e = 0; e < 4; e++) s.st[1][e] = n + SP1 * e;
    for (int e = 0; e < 8; e++) s.st[2][e] = TAG2 ? 98 + e + (e > 3 ? 1 : 0) + e / 2 : 98 + SP2 * e;
    s.fl[0] = FL0; s.fl[1] = FL1; s.fl[2] = TAG2 ? -1 : 150;
  }
  for (int role = 0; role < 2; role++) {
    int op[300] = {}, c = 0, mark = 0, idx[3] = {}, rm = 0;
    auto at = [&](int pos) { if (s.pc[pos] >= 0) op[c++] = s.pc[pos] / 2; };
    auto other = [&](int k) { while (k-- > 0) op[c++] = 99; };
    auto upto = [&](int qq) { int l = -1; for (int e = 0; e < c; e++) if (op[e] <= qq) l = e; return l < 0 || c - 1 - l > 62 ? 63 : c - 1 - l; };
    auto bar = [&](int n) { s.w[role][n + 1] = upto(cert(n)); };
    auto steps = [&](int n0, int n1) {
      for (int n = n0; n < n1; n++) {
        if (isbar(n)) bar(n);
        at(n);
        if (role == 0 && XST >= 0 && n >= 28 + XST && n < 28 + XST + 4 * XSP && (n - 28 - XST) % XSP == 0) { other(1); mark = c - 1; }
        if (role == 0 && n == 28 + XFS) { s.xw = c - 1 - mark; other(1); }
      }
    };
    if (role == 0) {
      for (int i = 0; i < 2 * PA; i++) op[c++] = i / 2;
      for (int i = 0; i < 28; i++) { at(DP(i)); if (i == 26) bar(-1); }
      bar(BA0); if (XST < 0) { other(PB ? 2 : 4); mark = c - 1; } if (BA1 >= 0) bar(BA1);
      bar(24); steps(28, 56);
      bar(56 + BM0); bar(56 + BM1); bar(80);
      s.w[0][85 + BM0] = s.w[0][85 + BM1] = s.w[0][109] = 63;
      s.pw = upto(27) - s.w[0][81];
    } else {
      other(PB ? 2 : 0);
      bar(-1); steps(0, 24);
      const int pw = c - 2;
      steps(24, 28);
      if (PB && pw < s.w[1][25]) s.w[1][25] = pw;
      bar(28 + BM0); bar(28 + BM1); bar(52); steps(56, 84);
      for (int t = 0; t < NG; t++) { at(GP(t)); if (t == G2A) bar(84 + BM0); if (t == G2B) bar(84 + BM1); if (t == G2C) bar(108); }
      for (int i = s.b0; i < s.b1; i++) { op[c++] = i; op[c++] = i; }
      for (int i = 0; i < 24; i++) if (s.rd[2 * i] < 0) s.gw[i] = upto(28 + i);
      for (int r = 0; r < 160; r++) {
        if (r == 112) { other(EARLY ? 4 : 0); s.rw = c - 1 - rm; }
        for (int i = 0; i < 24; i++) if (s.rd[2 * i] == r) s.gw[i] = upto(28 + i);
        if (r < 112) at(112 + r);
        if (r == OWN) s.ow = upto(55);
        for (int t = 0; t < 3; t++) for (int e = 0; e < (t < 2 || !TAG2 ? 4 : 8); e++) if (s.st[t][e] == r) { idx[t] = c; other(1); }
        for (int t = 0; t < 3; t++) if (s.fl[t] == r) { s.fw[t] = c - 1 - idx[t]; other(1); }
        if (r == XRES || r == XRES + 1) { other(2); rm = c - 1; }
        if (r == PPOLL || r == PP0) { other(1); mark = c - 1; }
        if (r == PC0) { s.cw = c - 1 - mark; other(4); }
      }
      s.qw = c - 1 - mark;
    }
  }
  s.late = q + (last < OWN - 6 && s.st[1][3] < 94 && s.st[0][3] < 63 ? 0 : 100);
  return s;
}
constexpr Sched SC = mksched();
static_assert(!(PB && XST >= 0) && XB >= 4 && XD <= XB && SC.late == 56 && SC.pw == 0 && LATE <= 3 && 2 * (PRE - PA) <= 25 - PS - (PS < 4) && FL0 > SC.st[0][3] && FL1 > SC.st[1][3] && XPOLL > 84 && XCHK > XPOLL && XCHK < 112 && YG <= OWN && SC.lag >= 12, "");
template <int ROLE, int N> DI void sbar() { if constexpr (N < 0 || isbar(N)) { if constexpr (SC.w[ROLE][N + 1] < 63) WAITV(SC.w[ROLE][N + 1]); BAR(); } }
DI void sy_set(int wv, int v) {
  unsigned t, u;
  asm volatile("s_mov_b64 exec, 1\n\tv_mov_b32 %0, %2\n\tv_mov_b32 %1, %3\n\tds_write_b32 %0, %1\n\ts_mov_b64 exec, -1" : "=&v"(t), "=&v"(u) : "s"(L_SY + wv * 4), "i"(v) : "memory");
}
DI u4 sy_get(int half) { unsigned z = L_SY + half * 16; TIE(z); return *(volatile LAS u4*)(uintptr_t)z; }
DI bool sy_ok(u4 v, unsigned n) { return __builtin_amdgcn_ballot_w64(min(min(v[0], v[1]), min(v[2], v[3])) < n) == 0; }
template <int SL = 0> DI void sy_wait(u4 v, int half, unsigned n) {
  if (__builtin_expect(!sy_ok(v, n), 0)) {
#pragma unroll 1
    for (int i = 0; i < 200000; i++) { __builtin_amdgcn_s_sleep(SL); if (sy_ok(sy_get(half), n)) break; }
  }
  asm volatile("" ::: "memory");
}

struct Stream {
  rsrc_t r1, r2;
  unsigned o1a, o1b, o2a, o2b, ring, so[7], s2[4], wa, wb;
  DI void init(const Params& p, int j, int rot, int a, int l) {
    r1 = mkrs(p.w1, 448 * 3584);
    const unsigned swz = ((l & 7) ^ (((l >> 4) & 1) << 2)) << 4;
    o1a = (16 * a + (l >> 3)) * 3584 + j * 512 + swz; o1b = o1a + 8 * 3584 - 1024;
    if (EXP & 4) { o1a = o1b = 0x7ffffff0u; }
    TIE(o1a); TIE(o1b);
    ring = L_RING + a * 2048;
    TIES(ring);
#pragma unroll
    for (int c = 0; c < 7; c++) { const int r = c + rot >= 7 ? c + rot - 7 : c + rot; so[c] = r * (64 * 3584); TIES(so[c]); }
    const int T = a >> 1, h = l >> 5, q = (l >> 2) & 3, pp = l & 3, dh = (l >> 4) & 1;
    wa = L_RING + (8 * h + q) * 128 + (((4 * T + 2 * dh + (pp >> 1)) ^ ((q >> 1) << 2)) << 4) + (pp & 1) * 8;
    wb = wa + 49152;
    TIE(wa); TIE(wb);
  }
  DI void init2(const Params& p, int j, int ur, int a, int l) {
    r2 = mkrs(p.w2, 1792 * 896);
    o2a = (256 * j + (l >> 2)) * 896 + (a >> 1) * 64 + (l & 3) * 16; o2b = o2a + 16 * 896 - 1024;
    if (EXP & 4) { o2a = o2b = 0x7ffffff0u; }
    TIE(o2a); TIE(o2b);
#pragma unroll
    for (int u = 0; u < 3; u++) { const int r = j + 2 * (u + ur >= 3 ? u + ur - 3 : u + ur) + 1 + (a & 1); s2[u] = (r >= 7 ? r - 7 : r) * 128; TIES(s2[u]); }
    s2[3] = j * 128 + (a & 1) * (32 * 896); TIES(s2[3]);
  }
  DI void second(int a, int l) {
    wa = L_RING + a * 2048 + (l >> 5) * 256 + ((l >> 2) & 3) * 64 + ((l >> 4) & 1) * 32 + (l & 3) * 8; wb = wa + 49152;
    TIE(wa); TIE(wb);
  }
  DI void shift(unsigned d) { wa += d; wb += d; TIE(wa); TIE(wb); }
  template <int Q, int O> DI bf4 rd() { return ldstr(((Q + 4) % 12 < 6 ? wa : wb) + ((Q + 4) % 6) * 8192 + O); }
  template <int Q> DI void m0() { setm0(ring, ((Q + 4) % 12) * 8192 - (Q < 28 ? (Q / 7) * 128 : 0)); }
  template <int Q, int H> DI void dma() {
    if constexpr (((EXP & 8) && Q >= PRE) || ((EXP & 256) && Q >= SC.b1) || ((EXP & 512) && Q >= 50)) return;
    else if constexpr (Q < 28) dma16(r1, H ? o1b : o1a, so[Q % 7], (Q / 7) * 128 + H * 1024);
    else if constexpr (Q < 52) dma16(r2, H ? o2b : o2a, s2[grp_u((Q - 28) >> 1)] + (2 * grp_q((Q - 28) >> 1) + (Q & 1)) * (32 * 896), H * 1024);
    else dma16(r2, H ? o2b : o2a, s2[3] + (Q - 52) * (64 * 896), H * 1024);
  }
  DI void burst() { sfor<SC.b0, SC.b1>([&](auto I_) AI { m0<I_.v>(); asm volatile("s_nop 0"); dma<I_.v, 0>(); dma<I_.v, 1>(); }); }
  template <int P> DI void pre() { if constexpr (SC.pc[P] >= 0 && !(SC.pc[P] & 1)) m0<SC.pc[P] / 2>(); }
  template <int P, bool PAD> DI void post() {
    if constexpr (SC.pc[P] >= 0) { if constexpr (PAD && !(SC.pc[P] & 1)) asm volatile("s_nop 0"); dma<SC.pc[P] / 2, SC.pc[P] & 1>(); }
  }
};
struct Norm {
  u4 sel, row, col;
  unsigned idx, b1[2], b2, ta;
  float rg;
  DI void init(const Params& p, int j, int ph, int a, int l) {
    const rsrc_t rb = mkrs(p.b1, 3584);
    asm volatile("buffer_load_ushort %0, %2, %3, %4 offen\n\tbuffer_load_ushort %1, %2, %3, %4 offen offset:256"
                 : "=&v"(b1[0]), "=&v"(b1[1]) : "v"((l & 31) * 2), "s"(rb), "s"(j * 512 + ph * 128 + (a >> 1) * 64) : "memory");
  }
  DI void consts(int a, int l) {
    const unsigned o = (l & 2) ? 0u : 0x3f803f80u, e = (l & 4) ? 0u : o;
    sel = u4{e, e, o ^ e, o ^ e}; idx = (l & 1) ? 0xeeeeu : 0x4444u;
    col = u4{0u, 0u, 0u, 0u}; row = col;
    ta = L_MT + (32 * (a & 1) + (l & 31)) * 4;
    TIE(sel); TIE(idx); TIE(col); TIE(row); TIE(ta);
  }
  float tm, ts;
  DI void read() { tm = *(const LAS float*)(uintptr_t)ta; ts = *(const LAS float*)(uintptr_t)(ta + 512); rg = *(const LAS float*)(uintptr_t)(ta + 768); }
  DI void rows(int l) {
    const unsigned a = __builtin_amdgcn_perm(__float_as_uint(tm), __float_as_uint(tm), 0x07060302u), b = __builtin_amdgcn_perm(__float_as_uint(ts), __float_as_uint(tm), 0x07060302u);
    row[0] = l < 32 ? a : 0u; row[1] = l < 32 ? b : 0u;
    TIE(row); TIE(rg);
  }
};
template <int P, class F> DI void m_phase(Stream& st, Norm& nm, f16v& acc, const bf8* X, F&& hook) {
  bf4 W[6][2];
  auto ld = [&](auto S_) AI { constexpr int s = S_.v; if constexpr (s < 28) { W[s % 6][0] = st.rd<7 * P + s / 4, (s & 3) * 2048>(); W[s % 6][1] = st.rd<7 * P + s / 4, (s & 3) * 2048 + 512>(); } };
  __builtin_amdgcn_s_setprio(MPRIO);
  sfor<0, 4>(ld);
  SCHED();
  f4 cs = {0.f, 0.f, 0.f, 0.f};
  f16v zero;
#pragma unroll
  for (int i = 0; i < 16; i++) zero[i] = 0.f;
  bf16v wp;
  float r0, r1;
  sfor<0, 28>([&](auto S_) AI {
    constexpr int s = S_.v, n = 28 * P + s;
    if constexpr (!(s & 1)) {
      wp = __builtin_shufflevector(cat(W[s % 6][0], W[s % 6][1]), cat(W[s % 6 + 1][0], W[s % 6 + 1][1]), 0, 1, 2, 3, 4, 5, 6, 7, 8, 9, 10, 11, 12, 13, 14, 15);
      cs = __builtin_amdgcn_smfmac_f32_16x16x64_bf16(asbf8(nm.sel), wp, cs, nm.idx, 0, 0);
      TIE(cs);
      SCHED();
      if constexpr (!isbar(n)) ld(IC<s + 4>{});
      SCHED();
      acc = mfma32(__builtin_shufflevector(wp, wp, 0, 1, 2, 3, 4, 5, 6, 7), X[s], s ? acc : zero);
    } else acc = mfma32(__builtin_shufflevector(wp, wp, 8, 9, 10, 11, 12, 13, 14, 15), X[s], acc);
    TIE(acc);
    SCHED();
    if constexpr (n == 108) WAITLK(0);
    if constexpr (isbar(n)) { sbar<!(P & 1), n>(); ld(IC<s + 4>{}); }
    if constexpr (!(s & 1)) { ld(IC<s + 5>{}); SCHED(); st.post<n, false>(); st.pre<n + 1>(); }
    SCHED();
    if constexpr (s & 1) st.post<n, false>();
    if constexpr (s == 23) __builtin_amdgcn_s_setprio(EPRIO);
    hook(S_);
    if constexpr (s == 26) { r0 = cs[0] + cs[1]; r1 = r0 - bfhi(__float_as_uint(r0)); nm.col[0] = __builtin_amdgcn_perm(__float_as_uint(r1), __float_as_uint(r0), 0x07060302u); TIE(r1); }
    if constexpr (s == 27) { r0 = r1 - bfhi(__float_as_uint(r1)); nm.col[1] = __builtin_amdgcn_perm(nm.b1[P >> 1], __float_as_uint(r0), 0x05040302u); }
    SCHED();
  });
  acc = mfma32(asbf8(nm.col), asbf8(nm.row), acc);
  TIE(acc);
  SCHED();
}
template <int P, int PRIO, class F> DI void g_phase(Stream& st, const f16v& acc, float rg, u4* hp, F&& hook) {
  __builtin_amdgcn_s_setprio(PRIO);
  float z[16], a[16];
  sfor<0, NG>([&](auto I_) AI {
    constexpr int t = I_.v - 1;
    if constexpr (P == 2) st.pre<GP(t + 1)>();
    if constexpr (P == 2 && t < 0) st.post<GP(0), true>();
    if constexpr (t >= 4 && t < 20) { z[t - 4] = __builtin_amdgcn_rcpf(z[t - 4]); SCHED(); }
    if constexpr (t >= 5 && t < 21) { z[t - 5] *= a[t - 5]; TIE(z[t - 5]); SCHED(); }
    if constexpr (t >= 3 && t < 19) { z[t - 3] = __builtin_fmaf(z[t - 3], GS, GS); SCHED(); }
    if constexpr (t >= 2 && t < 18) { z[t - 2] = __builtin_amdgcn_exp2f(z[t - 2]); SCHED(); }
    if constexpr (t >= 1 && t < 17) { z[t - 1] *= a[t - 1]; SCHED(); }
    if constexpr (t >= 0 && t < 16) { z[t] = __builtin_fmaf(a[t], a[t], GK); SCHED(); }
    if constexpr (t < 15) { a[t + 1] = acc[t + 1] * rg; SCHED(); }
    if constexpr (t >= 7 && (t & 1)) { unsigned v = pk(z[t - 7], z[t - 6]); TIE(v); hp[(t - 7) >> 3][((t - 7) >> 1) & 3] = v; SCHED(); }
    if constexpr (P == 2 && t >= 0) st.post<GP(t + 1), (t < 5)>();
    hook(I_);
    SCHED();
  });
}
DI void stp(u4 v, rsrc_t rs, unsigned voff, unsigned soff, int imm) {
  asm volatile("buffer_store_dwordx4 %0, %1, %2, %3 offen offset:%4" AS "\n\ts_nop 0" :: "v"(v), "v"(voff), "s"(rs), "s"(soff), "i"(imm) : "memory");
}
DI void put_h(u4 v, unsigned hw, int tile, int e) { *(LAS u4*)(uintptr_t)(hw + tile * 4096 + e * 1024) = v; }
DI void flagv(rsrc_t rs, unsigned off, u2 tag, unsigned fv) {
  asm volatile("buffer_store_dwordx2 %0, %1, %2, %3 offen" AG "" :: "v"(tag), "v"(fv), "s"(rs), "s"(off) : "memory");
}

DI void a_wave(const Params& p, int g, int j, int rot, int a, int l) {
  const u64 ep = epoch_load(p.ws);
  const int wv = a, m = a & 1, T = a >> 1, h = l >> 5, c = l & 31;
  const rsrc_t rws = mkrs(p.ws, WS_END);
  Stream st;
  Norm nm;
  auto touch = [&](int w) AI {
    const int e = (g * 4 + a) * 14 + l;
    if (w == 1) dma4(mkrs(p.w1, 448 * 3584), l < 14 ? (e >> 2) * 3584 + (e & 3) * 128 : 0x7ffffff0u, j * 512);
    else dma4(mkrs(p.w2, 1792 * 896), l < 14 ? e * 128 : 0x7ffffff0u, j * 229376);
  };
  auto warm = [&](int t) AI {
    if (TF == t && a == 3) dma4s(rws, l < 16 ? (HF && HF < 5 ? 0x7ffffff0u : WS_PF + (g * 7 + j) * 2048 + l * 128) : l < 18 ? WS_WF + (l & 1) * 128 : l == 18 ? WS_XF + j * 1024 + g * 32 : 0x7ffffff0u, 0, 0);
  };
  warm(0);
  if (T1 == 0) touch(1);
  if (T2 == 0) touch(2);
  nm.init(p, j, 1, a, l);
  SCHED();
  st.init(p, j, rot, a, l);
  sy_set(a, 0);
  f16v acc;
  const int row = 32 * m + c, k = 32 * j + g;
  const bool special = k == 0;
  u2 tag;
  unsigned xff;
  Fft ft;
  bf8 G[28], X[28];
  float gu[2][8], gc2;
  u4 y0, y1;
  unsigned xo, xs;
  auto frag = [&](auto N_) AI {
    constexpr int n = N_.v;
    float* v = gu[n & 1];
    if constexpr (n < 2) {
      const float gth = (float)(64 * j + 2 * c + T) * (1.0f / 448.0f);
#pragma unroll
      for (int i = 0; i < 8; i++) v[i] = cosr(__builtin_fmaf((float)(4 * h + 8 * n + (i >> 1)), gth, (i & 1) ? 0.25f : 0.f));
      if constexpr (n == 0) gc2 = 2.f * cosr(8.f * gth);
    } else {
#pragma unroll
      for (int i = 0; i < 8; i++) { v[i] = __builtin_fmaf(gc2, gu[(n & 1) ^ 1][i], -v[i]); TIE(v[i]); }
    }
    SCHED();
    G[n] = asbf8(u4{pk(v[0], v[1]), pk(v[2], v[3]), pk(v[4], v[5]), pk(v[6], v[7])});
    TIE(G[n]);
    SCHED();
  };
  {
    f16v acc1;
    float sgn, cs;
    bf8 F[14];
    int kf = 32 * j + c;
    float ph = T ? 0.25f : 0.f;
    if (kf == 0 && T) { kf = 224; ph = 0.f; }
    sgn = (kf & 1) ? -1.f : 1.f;
    const float th = (float)kf * (1.0f / 448.0f);
    ph = __builtin_fmaf((float)((kf * rot) % 7), 1.0f / 7.0f, ph);
#pragma unroll
    for (int i = 0; i < 16; i++) acc[i] = 0.f;
    warm(1);
    if (T1 == 1) touch(1);
    if (T2 == 1) touch(2);
    gen14(F, acc, __builtin_fmaf((float)(8 * h), th, ph), th, 16.f * th);
    if (T1 == 2) touch(1);
    if (T2 == 2) touch(2);
    warm(2);
    unsigned xa[4];
#pragma unroll
    for (int v = 0; v < 4; v++) { xa[v] = row * 128 + (((2 * v + h) ^ ((row >> 1) & 7)) << 4); TIE(xa[v]); }
    unsigned ma = L_MT + (32 * m + 4 * h) * 4;
    unsigned sgm = (kf & 1) ? 0x80008000u : 0u;
    TIE(ma);
    if (FS == 5) TIE(sgm); else TIE(sgn);
    SCHED();
    ft.consts(a, l);
    SCHED();
    if (TWF == 1 || TWF == 2) ft.twiddles<0>(a, l);
    if (TWF == 2) ft.twiddles<1>(a, l);
    if (TWF == 3) ft.tw0(a, l);
    SCHED();
    tag = epoch_tag(ep);
    xff = WS_XF + j * 1024 + g * 32 + a * 8;
    TIES(tag); TIES(xff);
    cs = (kf & 1) ? 0.f : 2.f * acc[0];
    TIE(cs);
    f16v zero;
#pragma unroll
    for (int i = 0; i < 16; i++) zero[i] = 0.f;
    auto ld = [&](auto S_) AI { constexpr int s = S_.v; if constexpr (s < 28) X[s] = lds128(xa[s & 3] + (s >> 2) * 8192); };
    if (T1 == 3) touch(1);
    if (T2 == 3) touch(2);
    warm(3);
    if constexpr (PA > 0) sfor<0, PA>([&](auto I_) AI { st.m0<I_.v>(); asm volatile("s_nop 0"); st.dma<I_.v, 0>(); st.dma<I_.v, 1>(); });
    BAR();
    sfor<0, 8>(ld);
    SCHED();
    f4 mm[4], rr[4];
    float v[16];
    sfor<0, 28>([&](auto S_) AI {
      constexpr int s = S_.v;
      if constexpr (s == 0) acc = mfma32(X[s], F[s], zero);
      else if constexpr (s < 14 || FS == 5) acc = mfma32(X[s], F[s % 14], acc);
      else if constexpr (s == 14) acc1 = mfma32(X[s], F[0], zero);
      else acc1 = mfma32(X[s], F[s - 14], acc1);
      if constexpr (s < 14 || FS == 5) TIE(acc); else TIE(acc1);
      SCHED();
      if constexpr (FS == 5 && s >= 2 && s < 16) { F[s - 2] = xor8(F[s - 2], sgm); TIE(F[s - 2]); }
      st.pre<DP(s)>();
      if constexpr (FS == 0 ? (s & 7) == 3 && s < 24 : (FS == 2 && s == 11) || (FS == 3 && s == 19) || (FS == 5 && s == SB)) BAR();
      if constexpr (FS == 5 && s == SB + 1) { for (int e = 0; e < 4; e++) mm[e] = ldsf4(ma + 32 * e); }
      if constexpr (FS == 5 && s == SB + 2) { for (int e = 0; e < 4; e++) rr[e] = ldsf4(ma + 256 + 32 * e); }
      if constexpr (FS == 5 && s >= 24) { for (int r = 4 * (s - 24); r < 4 * (s - 23); r++) { v[r] = mm[r >> 2][r & 3] * cs; TIE(v[r]); v[r] *= rr[r >> 2][r & 3]; TIE(v[r]); } }
      if constexpr (s & 4) { ld(IC<2 * s - (s & ~7)>{}); ld(IC<2 * s - (s & ~7) + 1>{}); }
      SCHED();
      st.post<DP(s), !((s & 4) && s < 22)>();
      if constexpr (TWF == 3 && s >= TW0 && s < TW0 + 16) ft.tw1<s - TW0>();
      if constexpr (s == 26) { sbar<0, -1>(); }
      SCHED();
    });
    __builtin_amdgcn_s_setprio(XPRIO);
    if (FS != 5) {
#pragma unroll
      for (int e = 0; e < 4; e++) mm[e] = ldsf4(ma + 32 * e);
#pragma unroll
      for (int e = 0; e < 4; e++) rr[e] = ldsf4(ma + 256 + 32 * e);
    }
    unsigned tw = L_Z + a * 2048 + c * 64 + ((h ^ (c >> 2)) & 3) * 16;
    TIE(tw);
    SCHED();
#pragma unroll
    for (int r = 0; r < 16; r++) if (FS != 5) { v[r] = __builtin_fmaf(mm[r >> 2][r & 3], cs, acc[r]); SCHED(); }
#pragma unroll
    for (int t = 0; t < 2; t++) {
#pragma unroll
      for (int r = 8 * t; r < 8 * t + 8; r++) { v[r] = FS == 5 ? __builtin_fmaf(acc[r], rr[r >> 2][r & 3], v[r]) : __builtin_fmaf(sgn, acc1[r], v[r]); SCHED(); }
#pragma unroll
      for (int r = 8 * t; r < 8 * t + 8; r++) { if (FS != 5) v[r] *= rr[r >> 2][r & 3]; TIE(v[r]); SCHED(); }
      const float* w = v + 8 * t;
      *(LAS u4*)(uintptr_t)(tw ^ (t * 32)) = u4{pk(w[0], w[1]), pk(w[2], w[3]), pk(w[4], w[5]), pk(w[6], w[7])};
      SCHED();
    }
    y0 = *(volatile LAS u4*)(uintptr_t)(L_Z + a * 2048 + l * 16); y1 = *(volatile LAS u4*)(uintptr_t)(L_Z + a * 2048 + 1024 + l * 16);
    xo = (l >> 2) * CSX + (((l ^ (l >> 4)) & 3) << 4); xs = WS_X1 + j * (32 * CSX) + g * 256 + m * 128 + T * 64;
    TIE(xo); TIES(xs);
    SCHED();
    sbar<0, BA0>();
    if (T2 == 4 || TF == 4) setm0n(0, L_DUM);
    if (T2 == 4) touch(2);
    warm(4);
    if (XST < 0) { stp(y0, rws, xo, xs, 0); stp(y1, rws, xo, xs + 16 * CSX, 0); }
  }
  u4 ones = {~0u, ~0u, ~0u, ~0u};
  unsigned vo = (l >> 4) * CSY + (l & 15) * 16, so = WS_Y1 + (32 * j + 8 * a) * CSY + g * GSY;
  if (PB) {}
  else if (XST < 0) { stp(ones, rws, vo, so, 0); stp(ones, rws, vo, so + 4 * CSY, 0); }
  else { TIE(ones); TIE(vo); TIES(so); }
  nm.consts(a, l);
  nm.read();
  if (TWF < 1) ft.twiddles<0>(a, l);
  if (TWF == 1) ft.twiddles<1>(a, l);
  SCHED();
  if constexpr (BA1 >= 0) sbar<0, BA1>();
  if (TWF < 1) ft.twiddles<1>(a, l);
  nm.rows(l);
  unsigned l16 = l * 16, hw = L_H + m * 16384 + T * 2048 + l16, fv = l ? 0x7ffffff0u : 0u;
  u2 tagv = tag;
  TIE(l16); TIE(hw); TIE(fv); TIE(tagv);
  SCHED();
  sbar<0, 24>();
  u4 hp[2];
  m_phase<1>(st, nm, acc, X, [&](auto S_) AI {
    if constexpr (XST >= 0 && S_.v == XST) stp(y0, rws, xo, xs, 0);
    if constexpr (XST >= 0 && S_.v == XST + XSP) stp(y1, rws, xo, xs + 16 * CSX, 0);
    if constexpr (XST >= 0 && S_.v == XST + 2 * XSP) stp(ones, rws, vo, so, 0);
    if constexpr (XST >= 0 && S_.v == XST + 3 * XSP) stp(ones, rws, vo, so + 4 * CSY, 0);
    if constexpr (S_.v == XFS) {
      asm volatile("s_waitcnt vmcnt(%2)" : "+v"(nm.b1[0]), "+v"(nm.b1[1]) : "i"(SC.xw) : "memory");
      flagv(rws, xff, tagv, fv);
    }
  });
  g_phase<1, GPRIO>(st, acc, nm.rg, hp, [&](auto I_) AI {
    if constexpr (I_.v == GB0) sbar<0, 56 + BM0>();
    if constexpr (I_.v == GB1) sbar<0, 56 + BM1>();
    if constexpr (I_.v == 15) put_h(hp[0], hw, 1, 0);
  });
  put_h(hp[1], hw, 1, 1);
  unsigned xfo = xflag_off(g, j, a, l), sm = (l & 1) ? 0x80008000u : 0u;
  TIE(xfo); TIE(sm);
  SCHED();
  sbar<0, 80>();
  u2 fl;
  bool ok;
  m_phase<3>(st, nm, acc, X, [&](auto S_) AI {
    constexpr int s = S_.v;
    if constexpr (s == XPOLL - 84) fl = ld8s(rws, xfo);
    if constexpr (s == XCHK - 84) {
      asm volatile("s_waitcnt vmcnt(0)" : "+v"(fl) :: "memory");
      ok = match(fl, tag);
      if (ok) fft_loads(rws, k, a, l);
    }
    if constexpr (s == 7) ft.addr(k, a, l);
    if constexpr (s >= 16 && s < 25) ft.expand<s - 16>(sm);
  });
  g_phase<3, FPRIO>(st, acc, nm.rg, hp, [&](auto I_) AI { if constexpr (I_.v == 15) put_h(hp[0], hw, 3, 0); });
  put_h(hp[1], hw, 3, 1);
  sy_set(a, 1);
  if (EXP & 16) sy_wait<8>(sy_get(1), 1, 2);
  if (!ok) {
    poll(rws, xfo, tag);
    fft_loads(rws, k, a, l);
  }
  WAITV(0);
  sy_set(a, 2);
  sy_wait(sy_get(0), 0, 2);
  __builtin_amdgcn_s_setprio(F2PRIO);
  ft.stageABC(a, l16, special);
  __builtin_amdgcn_s_setprio(F3PRIO);
  sy_set(a, 3);
  sy_wait(sy_get(0), 0, 3);
  ft.readD(l16);
  ft.mulD();
  ft.storeD(rws, special);
  sy_set(a, 4);
  const int q = (l >> 2) & 3, pp = l & 3, dh = (l >> 4) & 1;
  unsigned yo = (4 * a + (l >> 4)) * CSY + g * GSY + (((l & 15) ^ (((l >> 4) & 1) << 3)) << 4), yc = L_Y + a * 1024 + l16;
  TIE(yo); TIE(yc);
  __builtin_amdgcn_s_setprio(0);
  sfor<0, GF>(frag);
  sy_wait<2>(sy_get(0), 0, 4);
  sy_wait<2>(sy_get(1), 1, (EXP & 128) ? 4 : 2);
  auto ych = [&](auto N_) AI { setm0n(a * 1024, L_Y + N_.v * 4096); dma16s(rws, yo, WS_Y1 + N_.v * 16 * CSY); };
  sfor<0, 14>(ych);
  sfor<GF, 14>(frag);
  const unsigned gm = T ? ((c & 1) ? 0x80000000u : 0x00008000u) : ((c & 1) ? 0x80008000u : 0u);
  sfor<0, 14>([&](auto N_) AI { G[N_.v + 14] = xor8(G[N_.v], gm); TIE(G[N_.v + 14]); });
  if (h == 0) { u4 t = asu4(G[0]); t[0] = T ? 0xbf003f00u : 0x3f003f00u; G[0] = asbf8(t); }
  unsigned ya = L_Y + (4 * h + (q >> 1)) * 256 + (((8 * m + 4 * dh + pp) ^ ((q >> 1) << 3)) << 4) + (q & 1) * 8, yb = ya ^ (T * 8);
  TIE(G[0]); TIE(ya); TIE(yb);
  SCHED();
#pragma unroll 1
  for (int n = 0; n < SPIN_CAP; n++) {
    u4 d[14];
    unsigned e[14], all = 0;
    sfor<0, 3>([&](auto P_) AI {
      constexpr int i0 = P_.v == 0 ? 0 : P_.v == 1 ? YS : YS2, i1 = P_.v == 0 ? YS : P_.v == 1 ? YS2 : 14;
      WAITV(14 - i1);
#pragma unroll
      for (int i = i0; i < i1; i++) d[i] = *(volatile LAS u4*)(uintptr_t)(yc + i * 4096);
#pragma unroll
      for (int i = i0; i < i1; i++) { e[i] = max(d[i][0], d[i][2]); all = max(all, e[i]); }
      TIE(all);
    });
    if (__builtin_amdgcn_ballot_w64(all == ~0u) == 0) break;
    __builtin_amdgcn_s_sleep(YSL);
    sfor<0, 14>([&](auto N_) AI { if (__builtin_amdgcn_ballot_w64(e[N_.v] == ~0u) != 0) ych(N_); });
    WAITV(0);
  }
  sy_set(a, 5);
  sy_wait<1>(sy_get(0), 0, 5);
  {
    bf8 y[5];
    auto ld = [&](auto S_) AI { constexpr int s = S_.v; if constexpr (s < 28) { const unsigned b = (s < 14 ? ya : yb) + s * 2048; y[s % 5] = cat(ldstr(b), ldstr(b + 512)); } };
    sfor<0, 4>(ld);
#pragma unroll
    for (int i = 0; i < 16; i++) acc[i] = 0.f;
    sfor<0, 28>([&](auto S_) AI { acc = mfma32(G[S_.v], y[S_.v % 5], acc); SCHED(); ld(IC<S_.v + 4>{}); SCHED(); });
  }
#pragma unroll
  for (int e = 0; e < 4; e++) *(LAS f4*)(uintptr_t)(L_EX + a * 4096 + e * 1024 + l * 16) = f4{acc[4 * e], acc[4 * e + 1], acc[4 * e + 2], acc[4 * e + 3]};
  if (EXS) sy_set(a, 6);
  WAITL0();
  if (!EXS) BAR();
}

DI void b_wave(const Params& p, int g, int j, int rot, int wv, int l) {
  const int a = wv - 4, m = a & 1, T = a >> 1, h = l >> 5, c = l & 31;
  const rsrc_t rx = mkrs(p.x, 2048 * 896);
  unsigned xs[2];
#pragma unroll
  for (int e = 0; e < 2; e++) { const int row = 16 * a + 8 * e + (l >> 3); xs[e] = (32 * row + g) * 896 + (((l & 7) ^ ((row >> 1) & 7)) << 4) - e * 1024; }
  auto xch = [&](auto I_) AI {
    if constexpr (I_.v < 7) {
      const unsigned so = (I_.v + rot >= 7 ? I_.v + rot - 7 : I_.v + rot) * 128;
      setm0n(a * 2048, I_.v * 8192);
      asm volatile("buffer_load_dwordx4 %0, %2, %3 offen" AX " lds\n\tbuffer_load_dwordx4 %1, %2, %3 offen offset:1024" AX " lds" :: "v"(xs[0]), "v"(xs[1]), "s"(rx), "s"(so) : "memory");
      SCHED();
    }
  };
  if (!IB) sfor<0, XA>(xch);
  __builtin_amdgcn_s_setprio(IPRIO);
  const rsrc_t rws = mkrs(p.ws, WS_END);
  Stream st;
  Norm nm;
  st.init(p, j, rot, a, l);
  SCHED();
  if (IB == 2) sfor<0, XA>(xch);
  if (IB != 1) xch(IC<XA>{});
  st.init2(p, j, ROT & 2 ? ((g & 7) * 3) >> 3 : 0, a, l);
  nm.consts(a, l);
  sy_set(wv, 0);
  const int row = 32 * m + c;
  unsigned l16 = l * 16, fv = l ? 0x7ffffff0u : 0u, hw = L_H + m * 16384 + T * 2048 + l16;
  TIE(l16); TIE(fv); TIE(hw);
  SCHED();
  if (IB == 1) sfor<0, XA + 1>(xch);
  const u64 ep = epoch_load(p.ws);
  auto rest = [&]() AI {
    sfor<XA + 1, 7>(xch);
    nm.init(p, j, 0, a, l);
    asm volatile("buffer_load_ushort %0, %1, %2, %3 offen" : "=v"(nm.b2) : "v"(c * 2), "s"(mkrs(p.b2, 896)), "s"(j * 128 + T * 64) : "memory");
  };
  if (!XL) rest();
  const u2 tag = epoch_tag(ep);
  u2 tagv = tag;
  TIE(tagv);
  f16v acc[4];
  u4 hp[2];
  {
    bf8 X[28];
    unsigned xa[4];
    {
      f4 sm = {0.f, 0.f, 0.f, 0.f}, sq = {0.f, 0.f, 0.f, 0.f};
      const int r16 = l & 15, q = l >> 4, rw = 16 * a + r16;
      unsigned xp[2], mw = L_MT + q * 256 + rw * 4;
      float rs = (q & 2) ? GS : 2.0f / (2048.0f * 448.0f);
#pragma unroll
      for (int e = 0; e < 2; e++) { xp[e] = rw * 128 + (((q + 4 * ((r16 & 1) ^ e)) ^ ((rw >> 1) & 7)) << 4); TIE(xp[e]); }
#pragma unroll
      for (int v = 0; v < 4; v++) { xa[v] = row * 128 + (((2 * v + h) ^ ((row >> 1) & 7)) << 4); TIE(xa[v]); }
      bf8 ones = asbf8(u4{0x3f803f80u, 0x3f803f80u, 0x3f803f80u, 0x3f803f80u});
      u64 k0 = 0x1000010000100001ull, k1 = k0 << 1, k2 = k0 << 2, k3 = k0 << 3, q1 = 0xffff0000ffff0000ull, q2 = 0xffffffff00000000ull;
      TIE(mw); TIE(ones); TIE(rs); TIES(k0); TIES(k1); TIES(k2); TIES(k3); TIES(q1); TIES(q2);
      SCHED();
      if (XL) rest();
      __builtin_amdgcn_s_setprio(SPRIO);
      auto rdx = [&](int b) AI {
#pragma unroll
        for (int s = 8 * b; s < 8 * b + (b == 3 ? 4 : 8); s++) if (s < XB) X[s] = lds128(xa[s & 3] + (s >> 2) * 8192);
        SCHED();
      };
      sfor<0, 4>([&](auto B_) AI {
        constexpr int b = B_.v, nc = b == 3 ? 1 : 2;
        bf8 t[2][2];
        WAITV(b == 3 || (FS == 4 && b >= FB) ? 3 : 13 - 4 * b);
#pragma unroll
        for (int i = 0; i < nc; i++) { t[i][0] = lds128(xp[0] + (2 * b + i) * 8192); t[i][1] = lds128(xp[1] + (2 * b + i) * 8192); }
        SCHED();
        if constexpr (FS == 0 ? b < 2 : FS == 4 ? b == FB : FS > 1 && b == FS - 1 - (FS == 5)) BAR();
        if constexpr (b > 0 && !FS) rdx(b - 1);
        TIE(t[0][0]);
#pragma unroll
        for (int i = 0; i < nc; i++) {
          sq = mfma16(t[i][0], t[i][0], sq); sm = mfma16(ones, t[i][0], sm);
          sq = mfma16(t[i][1], t[i][1], sq); sm = mfma16(ones, t[i][1], sm);
          TIE(sq); TIE(sm);
        }
        SCHED();
        if constexpr (b == 2 && !FS) BAR();
      });
      float dg;
      asm volatile("s_nop 7\n\tv_cndmask_b32 %0, 0, %1, %5\n\tv_cndmask_b32 %0, %0, %2, %6\n\tv_cndmask_b32 %0, %0, %3, %7\n\tv_cndmask_b32 %0, %0, %4, %8"
                   : "=&v"(dg) : "v"(sq[0]), "v"(sq[1]), "v"(sq[2]), "v"(sq[3]), "s"(k0), "s"(k1), "s"(k2), "s"(k3));
      const float mean = sm[0] * (1.0f / 448.0f);
      u2 w = __builtin_amdgcn_permlane32_swap(__float_as_uint(dg), __float_as_uint(dg), false, false);
      dg = __uint_as_float(w[0]) + __uint_as_float(w[1]);
      w = __builtin_amdgcn_permlane16_swap(__float_as_uint(dg), __float_as_uint(dg), false, false);
      dg = __uint_as_float(w[0]) + __uint_as_float(w[1]);
      TIE(dg);
      SCHED();
      if (!FS) { BAR(); rdx(3); }
      const float var = __builtin_fmaxf(__builtin_fmaf(dg, 1.0f / 448.0f, -mean * mean), 0.f);
      const float sd = rbf(__builtin_amdgcn_sqrtf(rbf(rbf(var) + 1e-5f)));
      const float r = __builtin_amdgcn_rcpf(sd) * rs, nmean = rbf(-mean);
      float tv;
      asm volatile("v_cndmask_b32 %0, %1, %2, %4\n\tv_cndmask_b32 %0, %0, %3, %5" : "=&v"(tv) : "v"(nmean), "v"(sd), "v"(r), "s"(q2), "s"(q1));
      *(LAS float*)(uintptr_t)mw = tv;
      if (FS == 3) { rdx(0); rdx(1); rdx(2); }
      if (FS == 4) { rdx(0); rdx(1); rdx(2); rdx(3); }
      WAITL0();
      if (FS && FS != 4) {
        BAR();
        if (FS != 3) { rdx(0); rdx(1); rdx(2); }
        rdx(3);
        WAITL0();
      }
      if (PB) {
        asm volatile("s_waitcnt vmcnt(0)" : "+v"(nm.b1[0]), "+v"(nm.b1[1]), "+v"(nm.b2) :: "memory");
        const u4 ones = {~0u, ~0u, ~0u, ~0u};
        const unsigned vo = (l >> 4) * CSY + (l & 15) * 16, so = WS_Y1 + (32 * j + 8 * a) * CSY + g * GSY;
        __builtin_amdgcn_s_sleep(PBS);
        stp(ones, rws, vo, so, 0); stp(ones, rws, vo, so + 4 * CSY, 0);
      }
      BAR();
    }
    m_phase<0>(st, nm, acc[0], X, [&](auto S_) AI {
      constexpr int n = S_.v + XD;
      if constexpr (n >= XB && n < 28) X[n] = lds128(xa[n & 3] + (n >> 2) * 8192);
      if constexpr (S_.v == 11) nm.read();
      if constexpr (S_.v == 15) nm.rows(l);
      if constexpr (S_.v == 0 && !PB) asm volatile("s_waitcnt vmcnt(0)" : "+v"(nm.b1[0]), "+v"(nm.b1[1]), "+v"(nm.b2) :: "memory");
    });
    g_phase<0, GPRIO>(st, acc[0], nm.rg, hp, [&](auto I_) AI {
      if constexpr (I_.v == GB0) sbar<1, 28 + BM0>();
      if constexpr (I_.v == GB1) sbar<1, 28 + BM1>();
      if constexpr (I_.v == 15) put_h(hp[0], hw, 0, 0);
    });
    put_h(hp[1], hw, 0, 1);
    sbar<1, 52>();
    m_phase<2>(st, nm, acc[0], X, [&](auto S_) AI {});
  }
  g_phase<2, GPRIO>(st, acc[0], nm.rg, hp, [&](auto I_) AI {
    if constexpr (I_.v == G2A) sbar<1, 84 + BM0>();
    if constexpr (I_.v == G2B) sbar<1, 84 + BM1>();
    if constexpr (I_.v == G2C) { sbar<1, 108>(); }
    if constexpr (I_.v == 15) put_h(hp[0], hw, 2, 0);
    if constexpr (I_.v == 20) st.second(a, l);
  });
  put_h(hp[1], hw, 2, 1);
  sy_set(wv, 1);
  st.burst();
  unsigned pfo, pi, xo, po[2], pf[3], fw = HF < 3 ? fv : l < (HF == 3 || HF == 5 ? 16 : 8) ? l * 8 : 0x7ffffff0u;
  u2 fl, xr[4];
  u4 sl[2], pc[6][4];
  bool ok0 = false;
  {
    bf4 W[48][2], V[5][2];
    bf8 H[5];
    u4 tg[2], sy;
    f16v zero;
#pragma unroll
    for (int i = 0; i < 16; i++) zero[i] = 0.f;
    const unsigned ha[2] = {hw - T * 2048, (hw - T * 2048) ^ 49152};
    auto ld = [&](auto R_) AI {
      constexpr int r = R_.v, n = r + 4;
      sfor<0, 48>([&](auto P_) AI {
        constexpr int f = P_.v;
        if constexpr (SC.rd[f] == r) {
          if constexpr (!(f & 1) && SC.gw[f >> 1] < 63) WAITV(SC.gw[f >> 1]);
          W[f][0] = st.rd<28 + (f >> 1), (f & 1) * 1024>(); W[f][1] = st.rd<28 + (f >> 1), (f & 1) * 1024 + 512>();
        }
      });
      if constexpr (n >= 0 && n < 96) H[n % 5] = lds128(ha[n & 1] + (4 * grp_q(n >> 3) + ((n >> 1) & 3)) * 1024);
      if constexpr (n >= 96 && n < 112) H[n % 5] = lds128(ha[0] + (n & 15) * 1024);
      if constexpr (n == 96) st.shift(-m * 2048);
      if constexpr (n >= 96 && n < 112) { V[n % 5][0] = st.rd<28 + (n >> 2), (n & 3) * 1024>(); V[n % 5][1] = st.rd<28 + (n >> 2), (n & 3) * 1024 + 512>(); }
    };
    sfor<SC.rd[0], 0>(ld);
    __builtin_amdgcn_s_setprio(M2PRIO);
    SCHED();
    unsigned key, val;
    sfor<0, 112>([&](auto R_) AI {
      constexpr int r = R_.v, f = r >> 1;
      constexpr bool dbl = r < 96;
      constexpr int ks = dbl ? 4 * grp_q(r >> 3) + (f & 3) : r & 15, ai = dbl ? 2 * (grp_u(r >> 3) & 1) + (r & 1) : 2;
      if constexpr (dbl) acc[ai] = mfma32(cat(W[f][0], W[f][1]), H[r % 5], ks ? acc[ai] : zero);
      else acc[ai] = mfma32(cat(V[r % 5][0], V[r % 5][1]), H[r % 5], ks ? acc[ai] : zero);
      TIE(acc[ai]);
      SCHED();
      st.pre<112 + r>();
      if constexpr (r == 9 || r == 89) sy = sy_get(1);
      if constexpr (r == 17 + 8 * LATE) sy = sy_get(0);
      if constexpr (r == 12) sy_wait(sy, 1, 1);
      if constexpr (r == 20 + 8 * LATE) sy_wait(sy, 0, 1);
      if constexpr (r == 92) sy_wait(sy, 1, 3);
      ld(R_);
      SCHED();
      st.post<112 + r, true>();
      if constexpr (r == OWN) { WAITV(SC.ow); sy_set(wv, 3); }
      if constexpr (r == YG) sy_set(wv, 2);
      if constexpr ((EXP & 128) && r == 111) sy_set(wv, 4);
      if constexpr (TAG2 && (r == 93 || r == 95)) { tg[r == 95] = u4{0u, tag[0], 0u, tag[1]}; TIE(tg[r == 95]); }
      sfor<0, 24>([&](auto E_) AI {
        constexpr int t = E_.v >> 3, e = E_.v & 7;
        if constexpr (e == 0 && SC.st[t][0] - 3 == r) {
          const unsigned d = (g * 7 + (st.s2[t] >> 7)) * 4;
          po[0] = WS_P + (d * 6 + (2 * t + m) * 4 + a) * PSZ; po[1] = po[0] ^ PSZ;
          TIES(po[0]); TIES(po[1]);
          if constexpr (t < 2 || !TAG2) { pf[t] = WS_PF + (d + 2 * T) * 512 + (2 * t + m) * 128; TIES(pf[t]); }
        }
        constexpr bool tgd = TAG2 && t == 2;
        if constexpr (SC.st[t][e] == r && (tgd || e < 4)) {
          constexpr int hf = !tgd ? e >> 1 : e >> 2, x = e & 3;
          const f16v& pv = acc[2 * (t & 1) + hf];
          u4 v;
          if constexpr (!tgd) v = pack8(pv, 8 * (e & 1));
          else { tg[e & 1][0] = pk(pv[4 * x], pv[4 * x + 1]); tg[e & 1][2] = pk(pv[4 * x + 2], pv[4 * x + 3]); v = tg[e & 1]; }
          stp(v, rws, (EXP & 1) ? 0x7ffffff0u : l16, po[hf], (!tgd ? e & 1 : x) * 1024);
        }
      });
      sfor<0, 2>([&](auto T_) AI { if constexpr (SC.fl[T_.v] == r) { WAITV(SC.fw[T_.v]); flagv(rws, pf[T_.v], tagv, fw); } });
      if constexpr (r == 51) { xo = (32 * row + g) * 896 + j * 128 + T * 64 + h * 8; TIE(xo); }
      if constexpr (r == 53) { pfo = WS_PF + (g * 7 + j) * 2048 + T * 1024 + (PONE ? min(l & 7, 5) : l & 3) * 128; TIE(pfo); }
      if constexpr (r == 55) { pi = WS_P + ((g * 7 + j) * 24 + a) * PSZ + l16; TIE(pi); }
      if constexpr (r == 57) { key = (c >> 1) - 2 * h; val = 0x3f80u << (16 * (c & 1)); TIE(key); TIE(val); }
      if constexpr (r == XRES || r == XRES + 1) {
#pragma unroll
        for (int e = 2 * (r - XRES); e < 2 * (r - XRES) + 2; e++)
          asm volatile("buffer_load_dwordx2 %0, %1, %2, 0 offen offset:%3" : "=v"(xr[e]) : "v"(xo), "s"(rx), "i"(e * 16) : "memory");
      }
      if constexpr (r == PPOLL) fl = ld8s(rws, pfo);
      if constexpr (r == PP0) fl = ld8s(rws, pfo & ~256u);
      if constexpr (r == PC0) {
        asm volatile("s_waitcnt vmcnt(%1)" : "+v"(fl) : "i"(SC.cw) : "memory");
        ok0 = match(fl, tag);
        const unsigned pq = ok0 ? pi : 0x7ffffff0u;
#pragma unroll
        for (int e = 0; e < 4; e++)
          asm volatile("buffer_load_dwordx4 %0, %1, %2, %3 offen offset:%4" AL "" : "=v"(pc[e >> 1][e & 1]) : "v"(pq), "s"(rws), "s"((e >> 1) * 4 * PSZ), "i"((e & 1) * 1024) : "memory");
      }
      if constexpr (r >= 104) {
        constexpr int t = (r - 104) >> 2, e = r & 3;
        unsigned v = key == 8 * t + (e & 1) + 4 * (e >> 1) ? val : 0u;
        TIE(v); sl[t][e] = v;
      }
      SCHED();
    });
  }
  constexpr int NFL = TAG2 ? 4 : 6;
  f16v& out = acc[2];
  const bf8 sel0 = asbf8(sl[0]), sel1 = asbf8(sl[1]);
  auto ldt = [&](int r) AI {
#pragma unroll
    for (int e = 0; e < (r < NFL ? 2 : 4); e++)
      asm volatile("buffer_load_dwordx4 %0, %1, %2, %3 offen offset:%4" AL "" : "=v"(pc[r][e]) : "v"(pi), "s"(rws), "s"(r * 4 * PSZ), "i"(e * 1024) : "memory");
  };
  auto uset = [&](int r) AI {
    TIE(pc[r][0]); TIE(pc[r][1]);
    if (r < NFL) { out = mfma32(sel0, asbf8(pc[r][0]), out); out = mfma32(sel1, asbf8(pc[r][1]), out); TIE(out); return; }
#pragma unroll 1
    for (int n = 0; n < SPIN_CAP; n++) {
      TIE(pc[r][0]); TIE(pc[r][1]); TIE(pc[r][2]); TIE(pc[r][3]);
      if (match4(u4{pc[r][0][1], pc[r][0][3], pc[r][1][1], pc[r][1][3]}, u4{pc[r][2][1], pc[r][2][3], pc[r][3][1], pc[r][3][3]}, tag)) break;
      ldt(r);
      WAITV(0);
    }
    out = mfma32(sel0, asbf8(u4{pc[r][0][0], pc[r][0][2], pc[r][1][0], pc[r][1][2]}), out);
    out = mfma32(sel1, asbf8(u4{pc[r][2][0], pc[r][2][2], pc[r][3][0], pc[r][3][2]}), out);
    TIE(out);
  };
  if (EARLY) ldt(4);
  out = mfma32(asbf8(u4{nm.b2, 0u, 0u, 0u}), asbf8(u4{h ? 0u : 0x3f80u, 0u, 0u, 0u}), out);
  asm volatile("s_waitcnt vmcnt(%4)" : "+v"(xr[0]), "+v"(xr[1]), "+v"(xr[2]), "+v"(xr[3]) : "i"(SC.rw) : "memory");
  out = mfma32(sel0, asbf8(u4{xr[0][0], xr[0][1], xr[1][0], xr[1][1]}), out);
  out = mfma32(sel1, asbf8(u4{xr[2][0], xr[2][1], xr[3][0], xr[3][1]}), out);
  TIE(out);
  SCHED();
  sfor<0, 3>([&](auto T_) AI { if constexpr (SC.fl[T_.v] >= 112) { WAITV(SC.fw[T_.v]); flagv(rws, pf[T_.v], tagv, fw); } });
  unsigned ea = L_EX + m * 4096 + T * 2048 + c * 16 + h * 8;
  xo += h * 8;
  if (OPAT == 1) xo -= (c >> 4) * (16 * 32 * 896 - 32);
  unsigned tw = L_WT + a * 2048 + c * 64 + ((h ^ (c >> 2)) & 3) * 16, tr = L_WT + a * 2048 + l16;
  if (OPAT == 2) { xo = (32 * (32 * m + (l >> 2)) + g) * 896 + j * 128 + T * 64 + (((l ^ (l >> 4)) & 3) << 4); TIE(tw); TIE(tr); }
  rsrc_t ro = mkrs(p.out, 2048 * 896);
  TIE(ea); TIE(xo); TIES(ro);
  if constexpr (PPOLL < 112) {
    asm volatile("s_waitcnt vmcnt(%1)" : "+v"(fl) : "i"(SC.qw) : "memory");
    if (!match(fl, tag)) poll(rws, pfo, tag);
  } else {
    __builtin_amdgcn_s_sleep(PDEL);
    poll(rws, pfo, tag);
  }
  if (PC0 < 112) {
    if (!ok0) {
#pragma unroll
      for (int e = 0; e < 4; e++)
        asm volatile("buffer_load_dwordx4 %0, %1, %2, %3 offen offset:%4" AL "" : "+v"(pc[e >> 1][e & 1]) : "v"(pi), "s"(rws), "s"((e >> 1) * 4 * PSZ), "i"((e & 1) * 1024) : "memory");
    }
  } else { ldt(0); ldt(1); }
  ldt(2); ldt(3);
  if constexpr (TAG2) {
    if (!EARLY) ldt(4);
    ldt(5);
    WAITV(EARLY ? 4 : 8);
  } else {
    if (!PONE) poll(rws, pfo + 512 - (l & 2) * 128, tag);
    ldt(4); ldt(5);
    if (PONE) WAITV(4);
  }
#pragma unroll
  for (int r = 0; r < 4; r++) uset(r);
  if (!EXS) {
    WAITV(0);
    uset(4); uset(5);
    BAR();
  } else sy_wait<1>(sy_get(0), 0, 6);
#pragma unroll
  for (int e = 0; e < 4; e++)
#pragma unroll
    for (int t = 0; t < 2; t++) {
      const f2 v = *(const volatile LAS f2*)(uintptr_t)(ea + t * 8192 + (e >> 1) * 1024 + (e & 1) * 512);
      out[4 * e + t] += v[0]; out[4 * e + 2 + t] += v[1];
    }
  if (EXS) {
    TIE(out);
    WAITV(2);
    uset(4);
    WAITV(0);
    uset(5);
  }
  u4 z[2];
#pragma unroll
  for (int t = 0; t < 2; t++) {
    u4 v = pack8(out, 8 * t);
    u2 x = __builtin_amdgcn_permlane32_swap(v[0], v[2], false, false), y = __builtin_amdgcn_permlane32_swap(v[1], v[3], false, false);
    z[t] = u4{x[0], y[0], x[1], y[1]};
    if (!OPAT) __builtin_amdgcn_raw_buffer_store_b128(z[t], ro, xo + t * 32, 0, OAUX);
  }
  if (OPAT == 2) {
    *(volatile LAS u4*)(uintptr_t)tw = z[0]; *(volatile LAS u4*)(uintptr_t)(tw ^ 32) = z[1];
    z[0] = *(volatile LAS u4*)(uintptr_t)(tr); z[1] = *(volatile LAS u4*)(uintptr_t)(tr + 1024);
    __builtin_amdgcn_raw_buffer_store_b128(z[0], ro, xo, 0, OAUX);
    __builtin_amdgcn_raw_buffer_store_b128(z[1], ro, xo, 16 * 32 * 896, OAUX);
  }
  if (OPAT == 1) {
#pragma unroll
    for (int e = 0; e < 4; e++) { const u2 x = __builtin_amdgcn_permlane16_swap(z[0][e], z[1][e], false, false); z[0][e] = x[0]; z[1][e] = x[1]; }
    __builtin_amdgcn_raw_buffer_store_b128(z[0], ro, xo, 0, OAUX);
    __builtin_amdgcn_raw_buffer_store_b128(z[1], ro, xo, 16 * 32 * 896, OAUX);
  }
  if (blockIdx.x == 0 && a == 0) flagv(rws, 0, tagv, fv);
}

DI void helper(const Params& p, int id, int wv, int l) {
  const int tid = threadIdx.x;
  const rsrc_t rws = mkrs(p.ws, WS_END);
  const int w4 = id >> 3, f20 = (id & 7) * 4;
  {
    const unsigned to = l < 49 ? ((id * 8 + wv) * 49 + l) * 128 : 0x7ffffff0u;
    if (HF == 1) st8s(u2{0u, 0u}, rws, l < 14 ? WS_PF + ((id * 8 + wv) * 14 + l) * 128 + 8 : 0x7ffffff0u);
    if (T1 == 10) dma4(mkrs(p.w1, 448 * 3584), to, 0);
    if (T2 == 10) dma4(mkrs(p.w2, 448 * 3584), to, 0);
    if (HD) __builtin_amdgcn_s_sleep(HD);
    if (T2 == 11) dma4(mkrs(p.w2, 448 * 3584), to, 0);
    const rsrc_t rw = mkrs(wv >= 4 ? p.wi : p.wr, 2048 * 450);
    const unsigned so = (16 * w4 + 64 * (f20 + (wv & 3))) * 450;
#pragma unroll
    for (int n = 0; n < 8; n++) {
      const unsigned vo = (n < 7 || l < 2) ? n * 1024 + l * 16 : 0x7fffffffu;
      asm volatile("s_mov_b32 m0, %3\n\ts_nop 0\n\tbuffer_load_dwordx4 %0, %1, %2 offen nt lds" :: "v"(vo), "s"(rw), "s"(so), "s"(L_RING + wv * 8192 + n * 1024) : "memory");
    }
  }
  if (T2 == 12) { setm0n(0, L_DUM); dma4(mkrs(p.w2, 448 * 3584), l < 49 ? ((id * 8 + wv) * 49 + l) * 128 : 0x7ffffff0u, 0); }
  const u2 tag = epoch_tag(epoch_load(p.ws));
  const int c = tid & 15, t0 = tid >> 4;
  const unsigned se = (c & 1) ? 0x07060302u : 0x05040100u, so = se ^ 0x02020202u;
  const unsigned ra = L_RING + c * 450 - (c & 1) * 2 + t0 * 4;
  WAITV(T2 == 12);
  BAR();
#pragma unroll
  for (int it = 0; it < 4; it++) {
    const int t = t0 + 32 * it, ko = 2 * t + 1 - 2 * (c & 1);
    u4 v, w;
#pragma unroll
    for (int i = 0; i < 4; i++) {
      const unsigned a = *(const LAS unsigned*)(uintptr_t)(ra + i * 8192 + it * 128), b = *(const LAS unsigned*)(uintptr_t)(ra + 32768 + i * 8192 + it * 128);
      v[i] = __builtin_amdgcn_perm(b, a, se); w[i] = __builtin_amdgcn_perm(b, a, so);
    }
    const unsigned o = WS_WT + id * 256 + c * 16;
    st16s(v, rws, t <= 112 ? o + t * (2 * CSW) : 0x7ffffff0u, 0);
    st16s(w, rws, (unsigned)ko < 224u ? o + ko * CSW : 0x7ffffff0u, 0);
  }
  if (T2 == 13) { setm0n(0, L_DUM); dma4(mkrs(p.w2, 448 * 3584), l < 49 ? ((id * 8 + wv) * 49 + l) * 128 : 0x7ffffff0u, 0); }
  WAITV(T2 == 13);
  BAR();
  if (wv == 0) flag(rws, WS_WF + id * 8, tag, l);
  if (HF == 2) st8s(u2{0u, 0u}, rws, l < 14 ? WS_PF + ((id * 8 + wv) * 14 + l) * 128 + 8 : 0x7ffffff0u);
  if (T2 == 14) { setm0n(0, L_DUM); dma4(mkrs(p.w2, 448 * 3584), l < 49 ? ((id * 8 + wv) * 49 + l) * 128 : 0x7ffffff0u, 0); }
  if (T2 == 13 || T2 == 14) WAITV(0);
  if (id != 0 || wv >= 4) return;
  Fft ft;
  ft.consts(wv, l);
  ft.twiddles<0>(wv, l); ft.twiddles<1>(wv, l);
  sfor<0, 9>([&](auto I_) AI { ft.expand<I_.v>((l & 1) ? 0x80008000u : 0u); });
  ft.addr(224, wv, l);
  poll(rws, xflag_off(0, 0, wv, l), tag);
  fft_loads(rws, 224, wv, l);
  WAITV(0);
  BAR();
  ft.stageABC(wv, l * 16, true);
  WAITL0();
  BAR();
  ft.readD(l * 16);
  ft.mulD();
  ft.storeD(rws, true);
}

__global__ __launch_bounds__(512, 2) void k_fused(Params p) {
  u64 pc;
  if (KARG) asm volatile("s_load_dwordx2 %0, %1, 0x0" : "=s"(pc) : "s"(__builtin_amdgcn_kernarg_segment_ptr()));
  u64 k0 = pc;
  asm volatile("s_getpc_b64 %0" : "=s"(pc));
  const int b = blockIdx.x, xc = b & 7, i = b >> 3;
  const int l = threadIdx.x & 63, wv = __builtin_amdgcn_readfirstlane(threadIdx.x >> 6) ^ SWAP;
  {
    const rsrc_t rc = mkrs((const void*)(pc & ~127ull), CODE_LINES * 128);
    asm volatile("s_mov_b32 m0, %2\n\ts_nop 0\n\tbuffer_load_dword %0, %1, 0 offen lds" :: "v"(l * 32768 + (i * 8 + wv) * 128), "s"(rc), "i"(L_DUM) : "memory");
  }
  const int i3 = (i * 11) >> 5;
  const int j = i < 24 ? (xc & 1) * 4 + i - 3 * i3 : 3;
  const int g = (xc >> 1) * 8 + (i < 24 ? i3 : (xc & 1) * 4 + i - 24);
  if (KARG) asm volatile("s_waitcnt lgkmcnt(0)" :: "s"(k0));
  if (__builtin_expect(i >= 28, 0)) { helper(p, xc * 4 + (i - 28), wv, l); return; }
  int rot = (ROT & 4) ? 2 * (j & 3) : j;
  if (ROT & 1) { rot += g & 7; rot = rot >= 7 ? rot - 7 : rot; }
  if (__builtin_expect(wv >= 4, 1)) b_wave(p, g, j, rot, wv, l); else a_wave(p, g, j, rot, wv, l);
}

extern "C" void kernel_launch(void* const* d_in, const int* in_sizes, int n_in, void* d_out, int out_size, void* d_ws, size_t ws_size, hipStream_t stream) {
  Params p;
  p.x = (const char*)d_in[0]; p.wr = (const char*)d_in[1]; p.wi = (const char*)d_in[2]; p.w1 = (const char*)d_in[4];
  p.b1 = (const char*)d_in[5]; p.w2 = (const char*)d_in[6]; p.b2 = (const char*)d_in[7]; p.out = (char*)d_out; p.ws = (char*)d_ws;
  (void)hipFuncSetAttribute((const void*)k_fused, hipFuncAttributeMaxDynamicSharedMemorySize, L_TOTAL);
  k_fused<<<256, 512, L_TOTAL, stream>>>(p);
}
```
